```python
import torch

def _softmax(x, axis=-1):
    return torch.softmax(x, dim=axis)
def _gelu(x):
    return torch.nn.functional.gelu(x, approximate="tanh")
def _silu(x):
    return torch.nn.functional.silu(x)
def _sigmoid(x):
    return torch.sigmoid(x)
def _layernorm(x, axis=-1, eps=1e-5):
    m = x.mean(dim=axis, keepdim=True)
    v = x.var(dim=axis, keepdim=True, unbiased=False)
    return (x - m) / torch.sqrt(v + eps)
def _scatter_add(tgt, idx, src, axis=0):
    return tgt.clone().index_add_(axis, idx, src)
def _segment_sum(src, seg, n):
    out = torch.zeros((n,) + src.shape[1:], dtype=src.dtype)
    return out.index_add_(0, seg, src)
def _pad(x, pw):
    flat = tuple(v for pair in reversed(pw) for v in pair)
    return torch.nn.functional.pad(x, flat)

kernel_name = 'concat_heads__ssm_conv_s4__softmax_full_mha__prompt'

def setup_inputs():

    _w = (lambda a: (lambda t: t.to(torch.bfloat16) if t.is_floating_point() else t)(torch.as_tensor(a)))
    def _A_setup():
        B, L, D, N = 1, 2048, 576, 128
        A_re = -(lambda _x: torch.exp(torch.as_tensor(_x)))((lambda *s: torch.randn(*s, dtype=torch.bfloat16) * 6.4)(D, N))
        A_im = torch.pi * (lambda *s: torch.randn(*s, dtype=torch.bfloat16) * 0.1)(D, N)
        B_re = (lambda *s: torch.randn(*s, dtype=torch.bfloat16) * 0.2)(D, N)
        B_im = (lambda *s: torch.randn(*s, dtype=torch.bfloat16) * 0.2)(D, N)
        C_re = (lambda *s: torch.randn(*s, dtype=torch.bfloat16) * 0.2)(D, N)
        C_im = (lambda *s: torch.randn(*s, dtype=torch.bfloat16) * 0.2)(D, N)
        log_dt = (lambda *s: torch.randn(*s, dtype=torch.bfloat16) * 0.1)(D) * 0.5 - 4.0
        Dskip = (lambda *s: torch.randn(*s, dtype=torch.bfloat16) * 0.1)(D)
        return {
            "u": _w((lambda *s: torch.randn(*s, dtype=torch.bfloat16) * 0.05)(B, L, D)),
            "A_re": _w(A_re),
            "A_im": _w(A_im),
            "B_re": _w(B_re),
            "B_im": _w(B_im),
            "C_re": _w(C_re),
            "C_im": _w(C_im),
            "log_dt": _w(log_dt),
            "Dskip": _w(Dskip),
        }
    _A_in = _A_setup()

    def _B_setup():
        B, S, D = 1, 2048, 576
        return {
            "x": _w((lambda *s: torch.randn(*s, dtype=torch.bfloat16) * 0.2)(B, S, D)),
            "wqkv": _w((lambda *s: torch.randn(*s, dtype=torch.bfloat16) * 0.2)(D, 3 * D)),
            "wo": _w((lambda *s: torch.randn(*s, dtype=torch.bfloat16) * 0.05)(D, D)),
        }
    _B_in = _B_setup()

    return {
        "A_u": _A_in["u"],
        "A_A_re": _A_in["A_re"],
        "A_A_im": _A_in["A_im"],
        "A_B_re": _A_in["B_re"],
        "A_B_im": _A_in["B_im"],
        "A_C_re": _A_in["C_re"],
        "A_C_im": _A_in["C_im"],
        "A_log_dt": _A_in["log_dt"],
        "A_Dskip": _A_in["Dskip"],
        "B_x": _B_in["x"],
        "B_wqkv": _B_in["wqkv"],
        "B_wo": _B_in["wo"],
    }

def reference(A_u, A_A_re, A_A_im, A_B_re, A_B_im, A_C_re, A_C_im, A_log_dt, A_Dskip, B_x, B_wqkv, B_wo):
    def _A_ref(u, A_re, A_im, B_re, B_im, C_re, C_im, log_dt, Dskip):
        Bsz, L, D = u.shape
        dt = torch.exp(log_dt)[:, None]
        A = A_re + 1j * A_im
        Bc = B_re + 1j * B_im
        Cc = C_re + 1j * C_im
        logdA = dt * A
        dB = dt * Bc
        l = (lambda x, dt: x.to(dt))(torch.arange(L), A_re.dtype)
        P = torch.exp(logdA[:, :, None] * l[None, None, :])
        K = torch.einsum("dn,dnl,dn->dl", Cc, P, dB).real
        L2 = 2 * L
        Kp = _pad(K, ((0, 0), (0, L)))
        up = (lambda x, *d: x.permute(*d))(u, 0, 2, 1)
        up = _pad(up, ((0, 0), (0, 0), (0, L)))
        Kf = (lambda x, axes=(-2,-1): torch.fft.rfft2(x.float(), dim=axes))(Kp[:, None, :], axes=(-2, -1))
        Uf = (lambda x, axes=(-2,-1): torch.fft.rfft2(x.float(), dim=axes))(up[:, :, None, :], axes=(-2, -1))
        Yf = Uf * Kf[None, :, :, :]
        y = (lambda x, s, axes=(-2,-1): torch.fft.irfft2(x, s=s, dim=axes).to(torch.bfloat16))(Yf, (1, L2), axes=(-2, -1))[:, :, 0, :L]
        y = (lambda x, *d: x.permute(*d))(y, 0, 2, 1) + u * Dskip[None, None, :]
        return y

    def _B_ref(x, wqkv, wo):
        B, S, D = x.shape
        H = 32; Dh = D // H
        qkv = (lambda x, *d: x.permute(*d))((x @ wqkv).reshape(B, S, 3, H, Dh), 2, 0, 3, 1, 4)
        q, k, v = qkv[0], qkv[1], qkv[2]
        scale = Dh ** -0.5
        scores = torch.einsum("bhqd,bhkd->bhqk", q, k) * scale
        p = _softmax(scores, axis=-1)
        o = torch.einsum("bhqk,bhkd->bhqd", p, v)
        return (lambda x, *d: x.permute(*d))(o, 0, 2, 1, 3).reshape(B, S, D) @ wo

    a = _A_ref(A_u, A_A_re, A_A_im, A_B_re, A_B_im, A_C_re, A_C_im, A_log_dt, A_Dskip)
    b = _B_ref(B_x, B_wqkv, B_wo)
    return torch.concatenate([a, b], axis=-1)
```

```cpp
#include <hip/hip_runtime.h>
#include <hip/hip_bf16.h>
#include <stdint.h>
typedef unsigned short u16;
constexpr int L = 2048, DM = 576, NS = 128, NH = 32, DH = 18;
constexpr size_t OFF_QF = 0;
constexpr size_t OFF_QX = OFF_QF + 32 * 64 * 1024;
constexpr size_t CHUNK = 19456;
constexpr size_t OFF_KV = OFF_QX + 32 * 2048 * 4;
constexpr size_t OFF_O  = OFF_KV + 32 * 8 * CHUNK;
constexpr size_t OFF_UT = OFF_O + 2048 * 576 * 2;
constexpr int KRLEN = 2088;
constexpr size_t OFF_KR = OFF_UT + 576 * 2048 * 2;
constexpr size_t OFF_YT = OFF_KR + 576 * 2 * KRLEN * 2;

__device__ __forceinline__ float bf2f(u16 x) { return __uint_as_float((unsigned)x << 16); }
__device__ __forceinline__ u16 f2bf(float f) { return __builtin_bit_cast(u16, __float2bfloat16(f)); }
__device__ __forceinline__ int dim_main(int h, int s) { return s + 2 * (h & 1); }
__device__ __forceinline__ int dim_extra(int h, int e) { return (h & 1) ? e : 16 + e; }

__global__ void n_qkv(const u16* x, const u16* w, char* ws) {
    int idx = blockIdx.x * blockDim.x + threadIdx.x;
    if (idx >= 2048 * 1728) return;
    int t = idx / 1728, n = idx % 1728;
    float acc = 0;
    for (int k = 0; k < 576; k++) acc += bf2f(x[t * 576 + k]) * bf2f(w[k * 1728 + n]);
    int which = n / 576, h = (n % 576) / 18, dm = n % 18;
    int slot = -1, e = -1;
    if (h & 1) { if (dm < 2) e = dm; else slot = dm - 2; } else { if (dm < 16) slot = dm; else e = dm - 16; }
    if (which == 0) {
        acc *= 0.23570226039551584f * 1.4426950408889634f;
        if (slot >= 0) *(u16*)(ws + OFF_QF + ((size_t)(h * 64 + t / 32) * 64 + (slot >> 3) * 32 + (t & 31)) * 16 + (slot & 7) * 2) = f2bf(acc);
        else *(u16*)(ws + OFF_QX + ((size_t)h * 2048 + t) * 4 + e * 2) = f2bf(acc);
    } else {
        char* ch = ws + OFF_KV + ((size_t)h * 8 + t / 256) * CHUNK;
        int kk = t & 255;
        if (which == 1) {
            if (slot >= 0) *(u16*)(ch + (kk / 32) * 1024 + ((slot >> 3) * 32 + (kk & 31)) * 16 + (slot & 7) * 2) = f2bf(acc);
            else *(u16*)(ch + 8192 + kk * 4 + e * 2) = f2bf(acc);
        } else {
            int col = slot >= 0 ? slot : 16 + e;
            *(u16*)(ch + 9216 + kk * 40 + col * 2) = f2bf(acc);
            if (col == 17) { *(u16*)(ch + 9216 + kk * 40 + 36) = 0x3F80; *(u16*)(ch + 9216 + kk * 40 + 38) = 0; }
        }
    }
}
__device__ __forceinline__ int rho(int j) { return (int)bf2f(f2bf((float)j)); }
__global__ void n_kgen(const u16* Are, const u16* Aim, const u16* Bre, const u16* Bim, const u16* Cre, const u16* Cim, const u16* logdt, char* ws) {
    int idx = blockIdx.x * blockDim.x + threadIdx.x;
    if (idx >= 576 * 2089) return;
    int d = idx / 2089, x = idx % 2089;
    float val = 0;
    if (x <= 2047) {
        int j = 2047 - x; float l = (float)rho(j);
        float dt = bf2f(f2bf(expf(bf2f(logdt[d]))));
        for (int n = 0; n < 128; n++) {
            float a = dt * bf2f(Are[d * 128 + n]), b = dt * bf2f(Aim[d * 128 + n]);
            float br = dt * bf2f(Bre[d * 128 + n]), bi = dt * bf2f(Bim[d * 128 + n]);
            float cr = bf2f(Cre[d * 128 + n]), ci = bf2f(Cim[d * 128 + n]);
            float wr = cr * br - ci * bi, wi = cr * bi + ci * br;
            float ex = expf(a * l), s, c; sincosf(b * l, &s, &c);
            val += ex * (wr * c - wi * s);
        }
    }
    u16* g0 = (u16*)(ws + OFF_KR) + (size_t)d * 2 * KRLEN; u16* g1 = g0 + KRLEN;
    if (x < KRLEN) g0[x] = f2bf(val);
    if (x >= 1) g1[x - 1] = f2bf(val);
}
__global__ void n_ut(const u16* u, char* ws) {
    int idx = blockIdx.x * blockDim.x + threadIdx.x;
    if (idx >= 576 * 2048) return;
    int d = idx / 2048, l = idx % 2048;
    int c = l / 32, ks = (l >> 4) & 1, h = (l >> 3) & 1, j = l & 7;
    ((u16*)(ws + OFF_UT))[(((size_t)d * 4 + ks * 2 + h) * 64 + c) * 8 + j] = u[l * 576 + d];
}
__global__ void n_attn(char* ws) {
    int idx = blockIdx.x * blockDim.x + threadIdx.x;
    int h = idx / 2048, t = idx % 2048;
    float q[18];
    for (int s = 0; s < 16; s++) q[s] = bf2f(*(u16*)(ws + OFF_QF + ((size_t)(h * 64 + t / 32) * 64 + (s >> 3) * 32 + (t & 31)) * 16 + (s & 7) * 2));
    for (int e = 0; e < 2; e++) q[16 + e] = bf2f(*(u16*)(ws + OFF_QX + ((size_t)h * 2048 + t) * 4 + e * 2));
    float m = -1e30f, l = 0, o[18]; for (int i = 0; i < 18; i++) o[i] = 0;
    for (int k = 0; k < 2048; k++) {
        char* ch = ws + OFF_KV + ((size_t)h * 8 + k / 256) * CHUNK; int kk = k & 255;
        float s = 0;
        for (int i = 0; i < 16; i++) s += q[i] * bf2f(*(u16*)(ch + (kk / 32) * 1024 + ((i >> 3) * 32 + (kk & 31)) * 16 + (i & 7) * 2));
        for (int e = 0; e < 2; e++) s += q[16 + e] * bf2f(*(u16*)(ch + 8192 + kk * 4 + e * 2));
        float mn = fmaxf(m, s), al = exp2f(m - mn), p = exp2f(s - mn);
        l = l * al + p;
        for (int i = 0; i < 18; i++) o[i] = o[i] * al + p * bf2f(*(u16*)(ch + 9216 + kk * 40 + i * 2));
        m = mn;
    }
    u16* op = (u16*)(ws + OFF_O) + (size_t)t * 576;
    for (int hh = 0; hh < 2; hh++) for (int jj = 0; jj < 8; jj++) op[16 * h + 8 * hh + jj] = f2bf(o[(jj & 3) + 8 * (jj >> 2) + 4 * hh] / l);
    op[512 + 2 * h] = f2bf(o[16] / l); op[512 + 2 * h + 1] = f2bf(o[17] / l);
}
__global__ void n_conv(char* ws) {
    int idx = blockIdx.x * blockDim.x + threadIdx.x;
    int d = idx / 2048, i = idx % 2048;
    const u16* g0 = (const u16*)(ws + OFF_KR) + (size_t)d * 2 * KRLEN;
    const u16* ut = (const u16*)(ws + OFF_UT) + (size_t)d * 2048;
    float acc = 0;
    for (int j = 0; j <= i; j++) {
        int c = j / 32, ks = (j >> 4) & 1, h = (j >> 3) & 1, jj = j & 7;
        acc += bf2f(ut[((ks * 2 + h) * 64 + c) * 8 + jj]) * bf2f(g0[2047 - (i - j)]);
    }
    ((u16*)(ws + OFF_YT))[idx] = f2bf(acc);
}
__global__ void n_out(const u16* u, const u16* dskip, const u16* wo, const char* ws, u16* out) {
    int idx = blockIdx.x * blockDim.x + threadIdx.x;
    if (idx >= 2048 * 1152) return;
    int t = idx / 1152, c = idx % 1152;
    if (c < 576) {
        float y = bf2f(((const u16*)(ws + OFF_YT))[c * 2048 + t]);
        float s = bf2f(f2bf(bf2f(u[t * 576 + c]) * bf2f(dskip[c])));
        out[idx] = f2bf(y + s);
    } else {
        int n = c - 576; const u16* op = (const u16*)(ws + OFF_O) + (size_t)t * 576;
        float acc = 0;
        for (int kp = 0; kp < 576; kp++) {
            int h, dm;
            if (kp < 512) { h = kp >> 4; int hh = (kp >> 3) & 1, jj = kp & 7; dm = dim_main(h, (jj & 3) + 8 * (jj >> 2) + 4 * hh); }
            else { h = (kp - 512) >> 1; dm = dim_extra(h, kp & 1); }
            acc += bf2f(op[kp]) * bf2f(wo[(18 * h + dm) * 576 + n]);
        }
        out[idx] = f2bf(acc);
    }
}
extern "C" void kernel_launch(void* const* d_in, const int* in_sizes, int n_in, void* d_out, int out_size, void* d_ws, size_t ws_size, hipStream_t stream) {
    const u16* u = (const u16*)d_in[0];
    char* ws = (char*)d_ws;
    n_qkv<<<(2048 * 1728 + 255) / 256, 256, 0, stream>>>((const u16*)d_in[9], (const u16*)d_in[10], ws);
    n_kgen<<<(576 * 2089 + 255) / 256, 256, 0, stream>>>((const u16*)d_in[1], (const u16*)d_in[2], (const u16*)d_in[3], (const u16*)d_in[4], (const u16*)d_in[5], (const u16*)d_in[6], (const u16*)d_in[7], ws);
    n_ut<<<576 * 2048 / 256, 256, 0, stream>>>(u, ws);
    n_attn<<<32 * 2048 / 256, 256, 0, stream>>>(ws);
    n_conv<<<576 * 2048 / 256, 256, 0, stream>>>(ws);
    n_out<<<2048 * 1152 / 256, 256, 0, stream>>>(u, (const u16*)d_in[8], (const u16*)d_in[11], ws, (u16*)d_out);
}
```

```cpp
#define FUSED 1
#define NO_TOUCH1 1
#include <hip/hip_runtime.h>
#include <hip/hip_bf16.h>
#include <stdint.h>
#define NOPK __attribute__((target("no-packed-fp32-ops")))
typedef unsigned short u16;
typedef __attribute__((ext_vector_type(8))) short bf16x8;
typedef __attribute__((ext_vector_type(4))) short s16x4;
typedef __attribute__((ext_vector_type(4))) unsigned u32x4;
typedef __attribute__((ext_vector_type(2))) unsigned u32x2;
typedef __attribute__((ext_vector_type(16))) float f32x16;
typedef __attribute__((ext_vector_type(4))) float f32x4;
typedef __attribute__((address_space(3))) char* lds_ptr;
constexpr size_t CHUNK  = 9728;
constexpr size_t OFF_QF = 0;
constexpr size_t OFF_QX = OFF_QF + 32 * 64 * 1024;
constexpr size_t OFF_KV = OFF_QX + 32 * 2048 * 4;
constexpr size_t OFF_O  = OFF_KV + 32 * 16 * CHUNK;
constexpr size_t OFF_UT = OFF_O + 2048 * 576 * 2;
constexpr int    KRLEN  = 2088;
constexpr size_t OFF_KR = OFF_UT + 576 * 2048 * 2;
constexpr size_t OFF_YT = OFF_KR + 576 * 2 * KRLEN * 2;

#define MFMA32(a, b, c) __builtin_amdgcn_mfma_f32_32x32x16_bf16(__builtin_bit_cast(bf16x8, a), __builtin_bit_cast(bf16x8, b), c, 0, 0, 0)
#define MFMA16(a, b, c) __builtin_amdgcn_mfma_f32_16x16x32_bf16(__builtin_bit_cast(bf16x8, a), __builtin_bit_cast(bf16x8, b), c, 0, 0, 0)
__device__ __forceinline__ float bf2f(u16 x) { return __uint_as_float((unsigned)x << 16); }
__device__ __forceinline__ u16 f2bf(float f) { return __builtin_bit_cast(u16, __float2bfloat16(f)); }
typedef __attribute__((ext_vector_type(2))) float f32x2;
typedef __attribute__((ext_vector_type(2))) __bf16 bf16x2;
__device__ __forceinline__ unsigned cvtpk(float lo, float hi) { return __builtin_bit_cast(unsigned, __builtin_convertvector(f32x2{lo, hi}, bf16x2)); }
__device__ __forceinline__ unsigned rfl(unsigned x) { return __builtin_amdgcn_readfirstlane(x); }
__device__ __forceinline__ u32x4 make_srd(const void* p, unsigned bytes) {
    unsigned long long a = (unsigned long long)p;
    u32x4 s; s[0] = rfl((unsigned)a); s[1] = rfl((unsigned)(a >> 32)) & 0xffffu; s[2] = rfl(bytes); s[3] = 0x00020000u;
    return s;
}
__device__ __forceinline__ void dma16(u32x4 srd, unsigned voff, unsigned soff, unsigned m0dst) {
    unsigned keep;
    asm volatile("s_mov_b32 %0, m0\n\ts_mov_b32 m0, %4\n\ts_nop 0\n\tbuffer_load_dwordx4 %1, %2, %3 offen lds\n\ts_mov_b32 m0, %0"
                 : "=&s"(keep) : "v"(voff), "s"(srd), "s"(soff), "s"(m0dst) : "memory");
}
__device__ __forceinline__ void dma16h(u32x4 srd, unsigned voff, unsigned soff, unsigned m0dst) {
    unsigned keep;
    asm volatile("s_mov_b32 %0, m0\n\ts_mov_b32 m0, %4\n\ts_nop 0\n\tbuffer_load_dwordx4 %1, %2, %3 offen sc1 lds\n\ts_mov_b32 m0, %0"
                 : "=&s"(keep) : "v"(voff), "s"(srd), "s"(soff), "s"(m0dst) : "memory");
}
typedef short v4i16_t __attribute__((ext_vector_type(4)));
__device__ __forceinline__ u32x2 lds_tr(lds_ptr p) { return __builtin_bit_cast(u32x2, __builtin_amdgcn_ds_read_tr16_b64_v4i16((__attribute__((address_space(3))) v4i16_t*)p)); }
__device__ __forceinline__ u32x4 lds_r128(lds_ptr p) { return *(__attribute__((address_space(3))) const u32x4*)p; }
#define ST(i)
__device__ __forceinline__ __amdgpu_buffer_rsrc_t ws_rsrc(const void* p, unsigned bytes) { return __builtin_amdgcn_make_buffer_rsrc((void*)p, 0, bytes, 0x00020000); }
constexpr size_t OFF_YP = OFF_YT + 576 * 2048 * 2;
constexpr unsigned WS_USED = (unsigned)(OFF_YP + 64 * 3 * 2048 * 4);
#define STB blockIdx.x
#define FST(i)
#define HFST(i) FST(i)
typedef __attribute__((address_space(1))) unsigned gu32;
#define RLX_AGENT __ATOMIC_RELAXED, __HIP_MEMORY_SCOPE_AGENT
constexpr unsigned OFF_FL = (WS_USED + 255u) & ~255u;
constexpr unsigned FL_GEN = OFF_FL, FL_QKV = OFF_FL + 256  , FL_UT = FL_QKV + 16 * 128  , FL_KG = FL_UT + 9 * 256  ,
                   FL_CONV = FL_KG + 1024  , FL_ATT = FL_CONV + 9 * 128  , FL_LEFT = FL_ATT + 8 * 128  ;
__device__ __forceinline__ gu32* flag_ptr(char* ws, unsigned off) { return (gu32*)(ws + off); }
__device__ __forceinline__ unsigned read_tag(char* ws) { return __hip_atomic_load(flag_ptr(ws, FL_GEN), RLX_AGENT) + 1u; }
__device__ __forceinline__ void drain_bar() { asm volatile("s_waitcnt vmcnt(0) lgkmcnt(0)\n\ts_barrier" ::: "memory"); }
__device__ __forceinline__ void publish(char* ws, unsigned off, unsigned tag) { __hip_atomic_store(flag_ptr(ws, off), tag, RLX_AGENT); }
template <int SLEEP>
__device__ __forceinline__ void wait_flags(char* ws, unsigned off, unsigned tag) {
    if (threadIdx.x < 64) {
        for (unsigned spins = 0; spins < (1u << 20); ++spins) {
            const bool ok = off == 0 || __hip_atomic_load(flag_ptr(ws, off), RLX_AGENT) == tag;
            if (__all(ok)) break;
            __builtin_amdgcn_s_sleep(SLEEP);
        }
    }
    asm volatile("s_barrier" ::: "memory");
}
#define ATT_LOOP_ASM \
    "2:\n" \
    "v_exp_f32 v64, v64\n" \
    "v_exp_f32 v65, v65\n" \
    "v_mfma_f32_32x32x16_bf16 v[112:127], v[40:43], v[60:63], v[112:127]\n" \
    "ds_read_b128 v[40:43], %[va] offset:640\n" \
    "v_exp_f32 v66, v66\n" \
    "v_cvt_pk_bf16_f32 v56, v64, v65\n" \
    "v_exp_f32 v67, v67\n" \
    "v_exp_f32 v68, v68\n" \
    "v_cvt_pk_bf16_f32 v57, v66, v67\n" \
    "v_mfma_f32_32x32x16_bf16 v[80:95], v[52:55], v[32:35], 0\n" \
    "ds_read_b128 v[52:55], %[ka] offset:1024\n" \
    "v_exp_f32 v69, v69\n" \
    "v_exp_f32 v70, v70\n" \
    "v_cvt_pk_bf16_f32 v58, v68, v69\n" \
    "v_exp_f32 v71, v71\n" \
    "v_mfma_f32_32x32x16_bf16 v[80:95], v[48:51], v[24:27], v[80:95]\n" \
    "ds_read_b32 v48, %[xa] offset:128\n" \
    "v_exp_f32 v72, v72\n" \
    "v_cvt_pk_bf16_f32 v59, v70, v71\n" \
    "v_exp_f32 v73, v73\n" \
    "v_exp_f32 v74, v74\n" \
    "s_waitcnt lgkmcnt(3)\n" \
    "v_mfma_f32_32x32x16_bf16 v[96:111], v[44:47], v[56:59], v[96:111]\n" \
    "v_exp_f32 v75, v75\n" \
    "v_cvt_pk_bf16_f32 v60, v72, v73\n" \
    "v_exp_f32 v76, v76\n" \
    "v_exp_f32 v77, v77\n" \
    "v_cvt_pk_bf16_f32 v61, v74, v75\n" \
    "v_exp_f32 v78, v78\n" \
    "v_exp_f32 v79, v79\n" \
    "v_cvt_pk_bf16_f32 v62, v76, v77\n" \
    "s_waitcnt lgkmcnt(2)\n" \
    "v_cvt_pk_bf16_f32 v63, v78, v79\n" \
    "v_exp_f32 v80, v80\n" \
    "v_exp_f32 v81, v81\n" \
    "v_mfma_f32_32x32x16_bf16 v[96:111], v[40:43], v[60:63], v[96:111]\n" \
    "s_waitcnt lgkmcnt(1)\n" \
    "v_exp_f32 v82, v82\n" \
    "v_cvt_pk_bf16_f32 v56, v80, v81\n" \
    "v_exp_f32 v83, v83\n" \
    "v_exp_f32 v84, v84\n" \
    "v_cvt_pk_bf16_f32 v57, v82, v83\n" \
    "v_mfma_f32_32x32x16_bf16 v[64:79], v[52:55], v[36:39], 0\n" \
    "s_waitcnt lgkmcnt(0)\n" \
    "v_exp_f32 v85, v85\n" \
    "v_exp_f32 v86, v86\n" \
    "v_cvt_pk_bf16_f32 v58, v84, v85\n" \
    "v_exp_f32 v87, v87\n" \
    "v_mfma_f32_32x32x16_bf16 v[64:79], v[48:51], v[28:31], v[64:79]\n" \
    "v_exp_f32 v88, v88\n" \
    "v_cvt_pk_bf16_f32 v59, v86, v87\n" \
    "v_exp_f32 v89, v89\n" \
    "v_exp_f32 v90, v90\n" \
    "v_mfma_f32_32x32x16_bf16 v[112:127], v[44:47], v[56:59], v[112:127]\n" \
    "ds_read_b128 v[44:47], %[va] offset:1280\n" \
    "v_exp_f32 v91, v91\n" \
    "v_cvt_pk_bf16_f32 v60, v88, v89\n" \
    "v_exp_f32 v92, v92\n" \
    "v_exp_f32 v93, v93\n" \
    "v_cvt_pk_bf16_f32 v61, v90, v91\n" \
    "v_exp_f32 v94, v94\n" \
    "v_exp_f32 v95, v95\n" \
    "v_cvt_pk_bf16_f32 v62, v92, v93\n" \
    "s_nop 0\n" \
    "v_cvt_pk_bf16_f32 v63, v94, v95\n" \
    "s_waitcnt vmcnt(2)\n" \
    "s_barrier\n" \
    "v_exp_f32 v64, v64\n" \
    "v_exp_f32 v65, v65\n" \
    "v_mfma_f32_32x32x16_bf16 v[112:127], v[40:43], v[60:63], v[112:127]\n" \
    "ds_read_b128 v[40:43], %[va] offset:1920\n" \
    "v_exp_f32 v66, v66\n" \
    "v_cvt_pk_bf16_f32 v56, v64, v65\n" \
    "v_exp_f32 v67, v67\n" \
    "v_exp_f32 v68, v68\n" \
    "v_cvt_pk_bf16_f32 v57, v66, v67\n" \
    "v_mfma_f32_32x32x16_bf16 v[80:95], v[52:55], v[32:35], 0\n" \
    "ds_read_b128 v[52:55], %[ka] offset:2048\n" \
    "v_exp_f32 v69, v69\n" \
    "v_exp_f32 v70, v70\n" \
    "v_cvt_pk_bf16_f32 v58, v68, v69\n" \
    "v_exp_f32 v71, v71\n" \
    "v_mfma_f32_32x32x16_bf16 v[80:95], v[48:51], v[24:27], v[80:95]\n" \
    "ds_read_b32 v48, %[xa] offset:256\n" \
    "v_exp_f32 v72, v72\n" \
    "v_cvt_pk_bf16_f32 v59, v70, v71\n" \
    "v_exp_f32 v73, v73\n" \
    "v_exp_f32 v74, v74\n" \
    "s_waitcnt lgkmcnt(3)\n" \
    "v_mfma_f32_32x32x16_bf16 v[96:111], v[44:47], v[56:59], v[96:111]\n" \
    "v_exp_f32 v75, v75\n" \
    "v_cvt_pk_bf16_f32 v60, v72, v73\n" \
    "v_exp_f32 v76, v76\n" \
    "v_exp_f32 v77, v77\n" \
    "v_cvt_pk_bf16_f32 v61, v74, v75\n" \
    "v_exp_f32 v78, v78\n" \
    "v_exp_f32 v79, v79\n" \
    "v_cvt_pk_bf16_f32 v62, v76, v77\n" \
    "s_waitcnt lgkmcnt(2)\n" \
    "v_cvt_pk_bf16_f32 v63, v78, v79\n" \
    "v_exp_f32 v80, v80\n" \
    "v_exp_f32 v81, v81\n" \
    "v_mfma_f32_32x32x16_bf16 v[96:111], v[40:43], v[60:63], v[96:111]\n" \
    "s_waitcnt lgkmcnt(1)\n" \
    "v_exp_f32 v82, v82\n" \
    "v_cvt_pk_bf16_f32 v56, v80, v81\n" \
    "v_exp_f32 v83, v83\n" \
    "v_exp_f32 v84, v84\n" \
    "v_cvt_pk_bf16_f32 v57, v82, v83\n" \
    "v_mfma_f32_32x32x16_bf16 v[64:79], v[52:55], v[36:39], 0\n" \
    "s_waitcnt lgkmcnt(0)\n" \
    "v_exp_f32 v85, v85\n" \
    "v_exp_f32 v86, v86\n" \
    "v_cvt_pk_bf16_f32 v58, v84, v85\n" \
    "v_exp_f32 v87, v87\n" \
    "v_mfma_f32_32x32x16_bf16 v[64:79], v[48:51], v[28:31], v[64:79]\n" \
    "s_mov_b32 m0, %[lp0]\n" \
    "v_exp_f32 v88, v88\n" \
    "buffer_load_dwordx4 %[vo], %[srd], %[gp0] offen sc1 lds\n" \
    "v_cvt_pk_bf16_f32 v59, v86, v87\n" \
    "v_exp_f32 v89, v89\n" \
    "v_exp_f32 v90, v90\n" \
    "v_mfma_f32_32x32x16_bf16 v[112:127], v[44:47], v[56:59], v[112:127]\n" \
    "ds_read_b128 v[44:47], %[va] offset:2560\n" \
    "v_exp_f32 v91, v91\n" \
    "v_cvt_pk_bf16_f32 v60, v88, v89\n" \
    "v_exp_f32 v92, v92\n" \
    "v_exp_f32 v93, v93\n" \
    "v_cvt_pk_bf16_f32 v61, v90, v91\n" \
    "v_exp_f32 v94, v94\n" \
    "v_exp_f32 v95, v95\n" \
    "v_cvt_pk_bf16_f32 v62, v92, v93\n" \
    "s_nop 0\n" \
    "v_cvt_pk_bf16_f32 v63, v94, v95\n" \
    "v_exp_f32 v64, v64\n" \
    "v_exp_f32 v65, v65\n" \
    "v_mfma_f32_32x32x16_bf16 v[112:127], v[40:43], v[60:63], v[112:127]\n" \
    "ds_read_b128 v[40:43], %[va] offset:3200\n" \
    "v_exp_f32 v66, v66\n" \
    "v_cvt_pk_bf16_f32 v56, v64, v65\n" \
    "v_exp_f32 v67, v67\n" \
    "v_exp_f32 v68, v68\n" \
    "v_cvt_pk_bf16_f32 v57, v66, v67\n" \
    "v_mfma_f32_32x32x16_bf16 v[80:95], v[52:55], v[32:35], 0\n" \
    "ds_read_b128 v[52:55], %[ka] offset:3072\n" \
    "v_exp_f32 v69, v69\n" \
    "v_exp_f32 v70, v70\n" \
    "v_cvt_pk_bf16_f32 v58, v68, v69\n" \
    "v_exp_f32 v71, v71\n" \
    "v_mfma_f32_32x32x16_bf16 v[80:95], v[48:51], v[24:27], v[80:95]\n" \
    "ds_read_b32 v48, %[xa] offset:384\n" \
    "v_exp_f32 v72, v72\n" \
    "v_cvt_pk_bf16_f32 v59, v70, v71\n" \
    "v_exp_f32 v73, v73\n" \
    "v_exp_f32 v74, v74\n" \
    "s_waitcnt lgkmcnt(3)\n" \
    "v_mfma_f32_32x32x16_bf16 v[96:111], v[44:47], v[56:59], v[96:111]\n" \
    "v_exp_f32 v75, v75\n" \
    "v_cvt_pk_bf16_f32 v60, v72, v73\n" \
    "v_exp_f32 v76, v76\n" \
    "v_exp_f32 v77, v77\n" \
    "v_cvt_pk_bf16_f32 v61, v74, v75\n" \
    "v_exp_f32 v78, v78\n" \
    "v_exp_f32 v79, v79\n" \
    "v_cvt_pk_bf16_f32 v62, v76, v77\n" \
    "s_waitcnt lgkmcnt(2)\n" \
    "v_cvt_pk_bf16_f32 v63, v78, v79\n" \
    "v_exp_f32 v80, v80\n" \
    "v_exp_f32 v81, v81\n" \
    "v_mfma_f32_32x32x16_bf16 v[96:111], v[40:43], v[60:63], v[96:111]\n" \
    "s_waitcnt lgkmcnt(1)\n" \
    "v_exp_f32 v82, v82\n" \
    "v_cvt_pk_bf16_f32 v56, v80, v81\n" \
    "v_exp_f32 v83, v83\n" \
    "v_exp_f32 v84, v84\n" \
    "v_cvt_pk_bf16_f32 v57, v82, v83\n" \
    "v_mfma_f32_32x32x16_bf16 v[64:79], v[52:55], v[36:39], 0\n" \
    "s_waitcnt lgkmcnt(0)\n" \
    "v_exp_f32 v85, v85\n" \
    "v_exp_f32 v86, v86\n" \
    "v_cvt_pk_bf16_f32 v58, v84, v85\n" \
    "v_exp_f32 v87, v87\n" \
    "v_mfma_f32_32x32x16_bf16 v[64:79], v[48:51], v[28:31], v[64:79]\n" \
    "s_mov_b32 m0, %[lp1]\n" \
    "v_exp_f32 v88, v88\n" \
    "buffer_load_dwordx4 %[vo], %[srd], %[gp1] offen sc1 lds\n" \
    "v_cvt_pk_bf16_f32 v59, v86, v87\n" \
    "v_exp_f32 v89, v89\n" \
    "v_exp_f32 v90, v90\n" \
    "v_mfma_f32_32x32x16_bf16 v[112:127], v[44:47], v[56:59], v[112:127]\n" \
    "ds_read_b128 v[44:47], %[va] offset:3840\n" \
    "v_exp_f32 v91, v91\n" \
    "v_cvt_pk_bf16_f32 v60, v88, v89\n" \
    "v_exp_f32 v92, v92\n" \
    "v_exp_f32 v93, v93\n" \
    "v_cvt_pk_bf16_f32 v61, v90, v91\n" \
    "v_exp_f32 v94, v94\n" \
    "v_exp_f32 v95, v95\n" \
    "v_cvt_pk_bf16_f32 v62, v92, v93\n" \
    "s_nop 0\n" \
    "v_cvt_pk_bf16_f32 v63, v94, v95\n" \
    "v_exp_f32 v64, v64\n" \
    "v_exp_f32 v65, v65\n" \
    "v_mfma_f32_32x32x16_bf16 v[112:127], v[40:43], v[60:63], v[112:127]\n" \
    "s_setprio 0\n" \
    "ds_read_b128 v[40:43], %[va] offset:4480\n" \
    "v_exp_f32 v66, v66\n" \
    "v_cvt_pk_bf16_f32 v56, v64, v65\n" \
    "v_exp_f32 v67, v67\n" \
    "v_exp_f32 v68, v68\n" \
    "v_cvt_pk_bf16_f32 v57, v66, v67\n" \
    "v_mfma_f32_32x32x16_bf16 v[80:95], v[52:55], v[32:35], 0\n" \
    "ds_read_b128 v[52:55], %[ka] offset:20480\n" \
    "v_exp_f32 v69, v69\n" \
    "v_exp_f32 v70, v70\n" \
    "v_cvt_pk_bf16_f32 v58, v68, v69\n" \
    "v_exp_f32 v71, v71\n" \
    "v_mfma_f32_32x32x16_bf16 v[80:95], v[48:51], v[24:27], v[80:95]\n" \
    "ds_read_b32 v48, %[xa] offset:20480\n" \
    "v_exp_f32 v72, v72\n" \
    "v_cvt_pk_bf16_f32 v59, v70, v71\n" \
    "v_exp_f32 v73, v73\n" \
    "v_exp_f32 v74, v74\n" \
    "s_waitcnt lgkmcnt(3)\n" \
    "v_mfma_f32_32x32x16_bf16 v[96:111], v[44:47], v[56:59], v[96:111]\n" \
    "v_exp_f32 v75, v75\n" \
    "v_cvt_pk_bf16_f32 v60, v72, v73\n" \
    "v_exp_f32 v76, v76\n" \
    "v_exp_f32 v77, v77\n" \
    "v_cvt_pk_bf16_f32 v61, v74, v75\n" \
    "v_exp_f32 v78, v78\n" \
    "v_exp_f32 v79, v79\n" \
    "v_cvt_pk_bf16_f32 v62, v76, v77\n" \
    "s_waitcnt lgkmcnt(2)\n" \
    "v_cvt_pk_bf16_f32 v63, v78, v79\n" \
    "v_exp_f32 v80, v80\n" \
    "v_exp_f32 v81, v81\n" \
    "v_mfma_f32_32x32x16_bf16 v[96:111], v[40:43], v[60:63], v[96:111]\n" \
    "s_setprio 3\n" \
    "s_waitcnt lgkmcnt(1)\n" \
    "v_exp_f32 v82, v82\n" \
    "v_cvt_pk_bf16_f32 v56, v80, v81\n" \
    "v_exp_f32 v83, v83\n" \
    "v_exp_f32 v84, v84\n" \
    "v_cvt_pk_bf16_f32 v57, v82, v83\n" \
    "v_mfma_f32_32x32x16_bf16 v[64:79], v[52:55], v[36:39], 0\n" \
    "s_waitcnt lgkmcnt(0)\n" \
    "v_exp_f32 v85, v85\n" \
    "v_exp_f32 v86, v86\n" \
    "v_cvt_pk_bf16_f32 v58, v84, v85\n" \
    "v_exp_f32 v87, v87\n" \
    "v_mfma_f32_32x32x16_bf16 v[64:79], v[48:51], v[28:31], v[64:79]\n" \
    "v_exp_f32 v88, v88\n" \
    "v_cvt_pk_bf16_f32 v59, v86, v87\n" \
    "v_exp_f32 v89, v89\n" \
    "v_exp_f32 v90, v90\n" \
    "v_mfma_f32_32x32x16_bf16 v[112:127], v[44:47], v[56:59], v[112:127]\n" \
    "ds_read_b128 v[44:47], %[va] offset:20480\n" \
    "v_exp_f32 v91, v91\n" \
    "v_cvt_pk_bf16_f32 v60, v88, v89\n" \
    "v_exp_f32 v92, v92\n" \
    "v_exp_f32 v93, v93\n" \
    "v_cvt_pk_bf16_f32 v61, v90, v91\n" \
    "v_exp_f32 v94, v94\n" \
    "v_exp_f32 v95, v95\n" \
    "v_cvt_pk_bf16_f32 v62, v92, v93\n" \
    "s_nop 0\n" \
    "v_cvt_pk_bf16_f32 v63, v94, v95\n" \
    "v_exp_f32 v64, v64\n" \
    "v_exp_f32 v65, v65\n" \
    "v_mfma_f32_32x32x16_bf16 v[112:127], v[40:43], v[60:63], v[112:127]\n" \
    "ds_read_b128 v[40:43], %[va] offset:21120\n" \
    "v_exp_f32 v66, v66\n" \
    "v_cvt_pk_bf16_f32 v56, v64, v65\n" \
    "v_exp_f32 v67, v67\n" \
    "v_exp_f32 v68, v68\n" \
    "v_cvt_pk_bf16_f32 v57, v66, v67\n" \
    "v_mfma_f32_32x32x16_bf16 v[80:95], v[52:55], v[32:35], 0\n" \
    "ds_read_b128 v[52:55], %[ka] offset:21504\n" \
    "v_exp_f32 v69, v69\n" \
    "v_exp_f32 v70, v70\n" \
    "v_cvt_pk_bf16_f32 v58, v68, v69\n" \
    "v_exp_f32 v71, v71\n" \
    "v_mfma_f32_32x32x16_bf16 v[80:95], v[48:51], v[24:27], v[80:95]\n" \
    "ds_read_b32 v48, %[xa] offset:20608\n" \
    "v_exp_f32 v72, v72\n" \
    "v_cvt_pk_bf16_f32 v59, v70, v71\n" \
    "v_exp_f32 v73, v73\n" \
    "v_exp_f32 v74, v74\n" \
    "s_waitcnt lgkmcnt(3)\n" \
    "v_mfma_f32_32x32x16_bf16 v[96:111], v[44:47], v[56:59], v[96:111]\n" \
    "v_exp_f32 v75, v75\n" \
    "v_cvt_pk_bf16_f32 v60, v72, v73\n" \
    "v_exp_f32 v76, v76\n" \
    "v_exp_f32 v77, v77\n" \
    "v_cvt_pk_bf16_f32 v61, v74, v75\n" \
    "v_exp_f32 v78, v78\n" \
    "v_exp_f32 v79, v79\n" \
    "v_cvt_pk_bf16_f32 v62, v76, v77\n" \
    "s_waitcnt lgkmcnt(2)\n" \
    "v_cvt_pk_bf16_f32 v63, v78, v79\n" \
    "v_exp_f32 v80, v80\n" \
    "v_exp_f32 v81, v81\n" \
    "v_mfma_f32_32x32x16_bf16 v[96:111], v[40:43], v[60:63], v[96:111]\n" \
    "s_waitcnt lgkmcnt(1)\n" \
    "v_exp_f32 v82, v82\n" \
    "v_cvt_pk_bf16_f32 v56, v80, v81\n" \
    "v_exp_f32 v83, v83\n" \
    "v_exp_f32 v84, v84\n" \
    "v_cvt_pk_bf16_f32 v57, v82, v83\n" \
    "v_mfma_f32_32x32x16_bf16 v[64:79], v[52:55], v[36:39], 0\n" \
    "s_waitcnt lgkmcnt(0)\n" \
    "v_exp_f32 v85, v85\n" \
    "v_exp_f32 v86, v86\n" \
    "v_cvt_pk_bf16_f32 v58, v84, v85\n" \
    "v_exp_f32 v87, v87\n" \
    "v_mfma_f32_32x32x16_bf16 v[64:79], v[48:51], v[28:31], v[64:79]\n" \
    "v_exp_f32 v88, v88\n" \
    "v_cvt_pk_bf16_f32 v59, v86, v87\n" \
    "v_exp_f32 v89, v89\n" \
    "v_exp_f32 v90, v90\n" \
    "v_mfma_f32_32x32x16_bf16 v[112:127], v[44:47], v[56:59], v[112:127]\n" \
    "ds_read_b128 v[44:47], %[va] offset:21760\n" \
    "v_exp_f32 v91, v91\n" \
    "v_cvt_pk_bf16_f32 v60, v88, v89\n" \
    "v_exp_f32 v92, v92\n" \
    "v_exp_f32 v93, v93\n" \
    "v_cvt_pk_bf16_f32 v61, v90, v91\n" \
    "v_exp_f32 v94, v94\n" \
    "v_exp_f32 v95, v95\n" \
    "v_cvt_pk_bf16_f32 v62, v92, v93\n" \
    "s_nop 0\n" \
    "v_cvt_pk_bf16_f32 v63, v94, v95\n" \
    "s_waitcnt vmcnt(2)\n" \
    "s_barrier\n" \
    "v_exp_f32 v64, v64\n" \
    "v_exp_f32 v65, v65\n" \
    "v_mfma_f32_32x32x16_bf16 v[112:127], v[40:43], v[60:63], v[112:127]\n" \
    "ds_read_b128 v[40:43], %[va] offset:22400\n" \
    "v_exp_f32 v66, v66\n" \
    "v_cvt_pk_bf16_f32 v56, v64, v65\n" \
    "v_exp_f32 v67, v67\n" \
    "v_exp_f32 v68, v68\n" \
    "v_cvt_pk_bf16_f32 v57, v66, v67\n" \
    "v_mfma_f32_32x32x16_bf16 v[80:95], v[52:55], v[32:35], 0\n" \
    "ds_read_b128 v[52:55], %[ka] offset:22528\n" \
    "v_exp_f32 v69, v69\n" \
    "v_exp_f32 v70, v70\n" \
    "v_cvt_pk_bf16_f32 v58, v68, v69\n" \
    "v_exp_f32 v71, v71\n" \
    "v_mfma_f32_32x32x16_bf16 v[80:95], v[48:51], v[24:27], v[80:95]\n" \
    "ds_read_b32 v48, %[xa] offset:20736\n" \
    "v_exp_f32 v72, v72\n" \
    "v_cvt_pk_bf16_f32 v59, v70, v71\n" \
    "v_exp_f32 v73, v73\n" \
    "v_exp_f32 v74, v74\n" \
    "s_waitcnt lgkmcnt(3)\n" \
    "v_mfma_f32_32x32x16_bf16 v[96:111], v[44:47], v[56:59], v[96:111]\n" \
    "v_exp_f32 v75, v75\n" \
    "v_cvt_pk_bf16_f32 v60, v72, v73\n" \
    "v_exp_f32 v76, v76\n" \
    "v_exp_f32 v77, v77\n" \
    "v_cvt_pk_bf16_f32 v61, v74, v75\n" \
    "v_exp_f32 v78, v78\n" \
    "v_exp_f32 v79, v79\n" \
    "v_cvt_pk_bf16_f32 v62, v76, v77\n" \
    "s_waitcnt lgkmcnt(2)\n" \
    "v_cvt_pk_bf16_f32 v63, v78, v79\n" \
    "v_exp_f32 v80, v80\n" \
    "v_exp_f32 v81, v81\n" \
    "v_mfma_f32_32x32x16_bf16 v[96:111], v[40:43], v[60:63], v[96:111]\n" \
    "s_waitcnt lgkmcnt(1)\n" \
    "v_exp_f32 v82, v82\n" \
    "v_cvt_pk_bf16_f32 v56, v80, v81\n" \
    "v_exp_f32 v83, v83\n" \
    "v_exp_f32 v84, v84\n" \
    "v_cvt_pk_bf16_f32 v57, v82, v83\n" \
    "v_mfma_f32_32x32x16_bf16 v[64:79], v[52:55], v[36:39], 0\n" \
    "s_waitcnt lgkmcnt(0)\n" \
    "v_exp_f32 v85, v85\n" \
    "v_exp_f32 v86, v86\n" \
    "v_cvt_pk_bf16_f32 v58, v84, v85\n" \
    "v_exp_f32 v87, v87\n" \
    "v_mfma_f32_32x32x16_bf16 v[64:79], v[48:51], v[28:31], v[64:79]\n" \
    "s_mov_b32 m0, %[lp2]\n" \
    "v_exp_f32 v88, v88\n" \
    "buffer_load_dwordx4 %[vo], %[srd], %[gp2] offen sc1 lds\n" \
    "v_cvt_pk_bf16_f32 v59, v86, v87\n" \
    "v_exp_f32 v89, v89\n" \
    "v_exp_f32 v90, v90\n" \
    "v_mfma_f32_32x32x16_bf16 v[112:127], v[44:47], v[56:59], v[112:127]\n" \
    "ds_read_b128 v[44:47], %[va] offset:23040\n" \
    "v_exp_f32 v91, v91\n" \
    "v_cvt_pk_bf16_f32 v60, v88, v89\n" \
    "v_exp_f32 v92, v92\n" \
    "v_exp_f32 v93, v93\n" \
    "v_cvt_pk_bf16_f32 v61, v90, v91\n" \
    "v_exp_f32 v94, v94\n" \
    "v_exp_f32 v95, v95\n" \
    "v_cvt_pk_bf16_f32 v62, v92, v93\n" \
    "s_nop 0\n" \
    "v_cvt_pk_bf16_f32 v63, v94, v95\n" \
    "v_exp_f32 v64, v64\n" \
    "v_exp_f32 v65, v65\n" \
    "v_mfma_f32_32x32x16_bf16 v[112:127], v[40:43], v[60:63], v[112:127]\n" \
    "ds_read_b128 v[40:43], %[va] offset:23680\n" \
    "v_exp_f32 v66, v66\n" \
    "v_cvt_pk_bf16_f32 v56, v64, v65\n" \
    "v_exp_f32 v67, v67\n" \
    "v_exp_f32 v68, v68\n" \
    "v_cvt_pk_bf16_f32 v57, v66, v67\n" \
    "v_mfma_f32_32x32x16_bf16 v[80:95], v[52:55], v[32:35], 0\n" \
    "ds_read_b128 v[52:55], %[ka] offset:23552\n" \
    "v_exp_f32 v69, v69\n" \
    "v_exp_f32 v70, v70\n" \
    "v_cvt_pk_bf16_f32 v58, v68, v69\n" \
    "v_exp_f32 v71, v71\n" \
    "v_mfma_f32_32x32x16_bf16 v[80:95], v[48:51], v[24:27], v[80:95]\n" \
    "ds_read_b32 v48, %[xa] offset:20864\n" \
    "v_exp_f32 v72, v72\n" \
    "v_cvt_pk_bf16_f32 v59, v70, v71\n" \
    "v_exp_f32 v73, v73\n" \
    "v_exp_f32 v74, v74\n" \
    "s_waitcnt lgkmcnt(3)\n" \
    "v_mfma_f32_32x32x16_bf16 v[96:111], v[44:47], v[56:59], v[96:111]\n" \
    "v_exp_f32 v75, v75\n" \
    "v_cvt_pk_bf16_f32 v60, v72, v73\n" \
    "v_exp_f32 v76, v76\n" \
    "v_exp_f32 v77, v77\n" \
    "v_cvt_pk_bf16_f32 v61, v74, v75\n" \
    "v_exp_f32 v78, v78\n" \
    "v_exp_f32 v79, v79\n" \
    "v_cvt_pk_bf16_f32 v62, v76, v77\n" \
    "s_waitcnt lgkmcnt(2)\n" \
    "v_cvt_pk_bf16_f32 v63, v78, v79\n" \
    "v_exp_f32 v80, v80\n" \
    "v_exp_f32 v81, v81\n" \
    "v_mfma_f32_32x32x16_bf16 v[96:111], v[40:43], v[60:63], v[96:111]\n" \
    "s_waitcnt lgkmcnt(1)\n" \
    "v_exp_f32 v82, v82\n" \
    "v_cvt_pk_bf16_f32 v56, v80, v81\n" \
    "v_exp_f32 v83, v83\n" \
    "v_exp_f32 v84, v84\n" \
    "v_cvt_pk_bf16_f32 v57, v82, v83\n" \
    "v_mfma_f32_32x32x16_bf16 v[64:79], v[52:55], v[36:39], 0\n" \
    "s_waitcnt lgkmcnt(0)\n" \
    "v_exp_f32 v85, v85\n" \
    "v_exp_f32 v86, v86\n" \
    "v_cvt_pk_bf16_f32 v58, v84, v85\n" \
    "v_exp_f32 v87, v87\n" \
    "v_mfma_f32_32x32x16_bf16 v[64:79], v[48:51], v[28:31], v[64:79]\n" \
    "s_mov_b32 m0, %[lp3]\n" \
    "v_exp_f32 v88, v88\n" \
    "buffer_load_dwordx4 %[vo], %[srd], %[gp3] offen sc1 lds\n" \
    "v_cvt_pk_bf16_f32 v59, v86, v87\n" \
    "v_exp_f32 v89, v89\n" \
    "v_exp_f32 v90, v90\n" \
    "v_mfma_f32_32x32x16_bf16 v[112:127], v[44:47], v[56:59], v[112:127]\n" \
    "ds_read_b128 v[44:47], %[va] offset:24320\n" \
    "v_exp_f32 v91, v91\n" \
    "v_cvt_pk_bf16_f32 v60, v88, v89\n" \
    "v_exp_f32 v92, v92\n" \
    "v_exp_f32 v93, v93\n" \
    "v_cvt_pk_bf16_f32 v61, v90, v91\n" \
    "v_exp_f32 v94, v94\n" \
    "v_exp_f32 v95, v95\n" \
    "v_cvt_pk_bf16_f32 v62, v92, v93\n" \
    "s_nop 0\n" \
    "v_cvt_pk_bf16_f32 v63, v94, v95\n" \
    "v_exp_f32 v64, v64\n" \
    "v_exp_f32 v65, v65\n" \
    "v_mfma_f32_32x32x16_bf16 v[112:127], v[40:43], v[60:63], v[112:127]\n" \
    "s_setprio 0\n" \
    "ds_read_b128 v[40:43], %[va] offset:24960\n" \
    "v_exp_f32 v66, v66\n" \
    "v_cvt_pk_bf16_f32 v56, v64, v65\n" \
    "v_exp_f32 v67, v67\n" \
    "v_exp_f32 v68, v68\n" \
    "v_cvt_pk_bf16_f32 v57, v66, v67\n" \
    "v_mfma_f32_32x32x16_bf16 v[80:95], v[52:55], v[32:35], 0\n" \
    "ds_read_b128 v[52:55], %[ka2] offset:0\n" \
    "v_exp_f32 v69, v69\n" \
    "v_exp_f32 v70, v70\n" \
    "v_cvt_pk_bf16_f32 v58, v68, v69\n" \
    "v_exp_f32 v71, v71\n" \
    "v_mfma_f32_32x32x16_bf16 v[80:95], v[48:51], v[24:27], v[80:95]\n" \
    "ds_read_b32 v48, %[xa2] offset:0\n" \
    "v_exp_f32 v72, v72\n" \
    "v_cvt_pk_bf16_f32 v59, v70, v71\n" \
    "v_exp_f32 v73, v73\n" \
    "v_exp_f32 v74, v74\n" \
    "s_waitcnt lgkmcnt(3)\n" \
    "v_mfma_f32_32x32x16_bf16 v[96:111], v[44:47], v[56:59], v[96:111]\n" \
    "v_exp_f32 v75, v75\n" \
    "v_cvt_pk_bf16_f32 v60, v72, v73\n" \
    "v_exp_f32 v76, v76\n" \
    "v_exp_f32 v77, v77\n" \
    "v_cvt_pk_bf16_f32 v61, v74, v75\n" \
    "v_exp_f32 v78, v78\n" \
    "v_exp_f32 v79, v79\n" \
    "v_cvt_pk_bf16_f32 v62, v76, v77\n" \
    "s_waitcnt lgkmcnt(2)\n" \
    "v_cvt_pk_bf16_f32 v63, v78, v79\n" \
    "v_exp_f32 v80, v80\n" \
    "v_exp_f32 v81, v81\n" \
    "v_mfma_f32_32x32x16_bf16 v[96:111], v[40:43], v[60:63], v[96:111]\n" \
    "s_setprio 3\n" \
    "s_waitcnt lgkmcnt(1)\n" \
    "v_exp_f32 v82, v82\n" \
    "v_cvt_pk_bf16_f32 v56, v80, v81\n" \
    "v_exp_f32 v83, v83\n" \
    "v_exp_f32 v84, v84\n" \
    "v_cvt_pk_bf16_f32 v57, v82, v83\n" \
    "v_mfma_f32_32x32x16_bf16 v[64:79], v[52:55], v[36:39], 0\n" \
    "s_waitcnt lgkmcnt(0)\n" \
    "v_exp_f32 v85, v85\n" \
    "v_exp_f32 v86, v86\n" \
    "v_cvt_pk_bf16_f32 v58, v84, v85\n" \
    "v_exp_f32 v87, v87\n" \
    "v_mfma_f32_32x32x16_bf16 v[64:79], v[48:51], v[28:31], v[64:79]\n" \
    "s_mov_b32 m0, %[lp4]\n" \
    "v_exp_f32 v88, v88\n" \
    "buffer_load_dwordx4 %[vo], %[srd], %[gp4] offen sc1 lds\n" \
    "v_cvt_pk_bf16_f32 v59, v86, v87\n" \
    "v_exp_f32 v89, v89\n" \
    "v_exp_f32 v90, v90\n" \
    "v_mfma_f32_32x32x16_bf16 v[112:127], v[44:47], v[56:59], v[112:127]\n" \
    "ds_read_b128 v[44:47], %[va2] offset:0\n" \
    "v_exp_f32 v91, v91\n" \
    "v_cvt_pk_bf16_f32 v60, v88, v89\n" \
    "v_exp_f32 v92, v92\n" \
    "v_exp_f32 v93, v93\n" \
    "v_cvt_pk_bf16_f32 v61, v90, v91\n" \
    "v_exp_f32 v94, v94\n" \
    "v_exp_f32 v95, v95\n" \
    "v_cvt_pk_bf16_f32 v62, v92, v93\n" \
    "s_nop 0\n" \
    "v_cvt_pk_bf16_f32 v63, v94, v95\n" \
    "v_add_u32 %[vo], 0x4c00, %[vo]\n" \
    "v_exp_f32 v64, v64\n" \
    "v_exp_f32 v65, v65\n" \
    "v_mfma_f32_32x32x16_bf16 v[112:127], v[40:43], v[60:63], v[112:127]\n" \
    "ds_read_b128 v[40:43], %[va2] offset:640\n" \
    "v_exp_f32 v66, v66\n" \
    "v_cvt_pk_bf16_f32 v56, v64, v65\n" \
    "v_exp_f32 v67, v67\n" \
    "v_exp_f32 v68, v68\n" \
    "v_cvt_pk_bf16_f32 v57, v66, v67\n" \
    "v_mfma_f32_32x32x16_bf16 v[80:95], v[52:55], v[32:35], 0\n" \
    "ds_read_b128 v[52:55], %[ka2] offset:1024\n" \
    "v_exp_f32 v69, v69\n" \
    "v_exp_f32 v70, v70\n" \
    "v_cvt_pk_bf16_f32 v58, v68, v69\n" \
    "v_exp_f32 v71, v71\n" \
    "v_mfma_f32_32x32x16_bf16 v[80:95], v[48:51], v[24:27], v[80:95]\n" \
    "ds_read_b32 v48, %[xa2] offset:128\n" \
    "v_exp_f32 v72, v72\n" \
    "v_cvt_pk_bf16_f32 v59, v70, v71\n" \
    "v_exp_f32 v73, v73\n" \
    "v_exp_f32 v74, v74\n" \
    "s_waitcnt lgkmcnt(3)\n" \
    "v_mfma_f32_32x32x16_bf16 v[96:111], v[44:47], v[56:59], v[96:111]\n" \
    "v_exp_f32 v75, v75\n" \
    "v_cvt_pk_bf16_f32 v60, v72, v73\n" \
    "v_exp_f32 v76, v76\n" \
    "v_exp_f32 v77, v77\n" \
    "v_cvt_pk_bf16_f32 v61, v74, v75\n" \
    "v_exp_f32 v78, v78\n" \
    "v_exp_f32 v79, v79\n" \
    "v_cvt_pk_bf16_f32 v62, v76, v77\n" \
    "s_waitcnt lgkmcnt(2)\n" \
    "v_cvt_pk_bf16_f32 v63, v78, v79\n" \
    "v_exp_f32 v80, v80\n" \
    "v_exp_f32 v81, v81\n" \
    "v_mfma_f32_32x32x16_bf16 v[96:111], v[40:43], v[60:63], v[96:111]\n" \
    "s_waitcnt lgkmcnt(1)\n" \
    "v_exp_f32 v82, v82\n" \
    "v_cvt_pk_bf16_f32 v56, v80, v81\n" \
    "v_exp_f32 v83, v83\n" \
    "v_exp_f32 v84, v84\n" \
    "v_cvt_pk_bf16_f32 v57, v82, v83\n" \
    "v_mfma_f32_32x32x16_bf16 v[64:79], v[52:55], v[36:39], 0\n" \
    "s_waitcnt lgkmcnt(0)\n" \
    "v_exp_f32 v85, v85\n" \
    "v_exp_f32 v86, v86\n" \
    "v_cvt_pk_bf16_f32 v58, v84, v85\n" \
    "v_exp_f32 v87, v87\n" \
    "v_mfma_f32_32x32x16_bf16 v[64:79], v[48:51], v[28:31], v[64:79]\n" \
    "v_exp_f32 v88, v88\n" \
    "v_cvt_pk_bf16_f32 v59, v86, v87\n" \
    "v_exp_f32 v89, v89\n" \
    "v_exp_f32 v90, v90\n" \
    "v_mfma_f32_32x32x16_bf16 v[112:127], v[44:47], v[56:59], v[112:127]\n" \
    "ds_read_b128 v[44:47], %[va2] offset:1280\n" \
    "v_exp_f32 v91, v91\n" \
    "v_cvt_pk_bf16_f32 v60, v88, v89\n" \
    "v_exp_f32 v92, v92\n" \
    "v_exp_f32 v93, v93\n" \
    "v_cvt_pk_bf16_f32 v61, v90, v91\n" \
    "v_exp_f32 v94, v94\n" \
    "v_exp_f32 v95, v95\n" \
    "v_cvt_pk_bf16_f32 v62, v92, v93\n" \
    "s_nop 0\n" \
    "v_cvt_pk_bf16_f32 v63, v94, v95\n" \
    "s_waitcnt vmcnt(2)\n" \
    "s_barrier\n" \
    "v_exp_f32 v64, v64\n" \
    "v_exp_f32 v65, v65\n" \
    "v_mfma_f32_32x32x16_bf16 v[112:127], v[40:43], v[60:63], v[112:127]\n" \
    "ds_read_b128 v[40:43], %[va2] offset:1920\n" \
    "v_exp_f32 v66, v66\n" \
    "v_cvt_pk_bf16_f32 v56, v64, v65\n" \
    "v_exp_f32 v67, v67\n" \
    "v_exp_f32 v68, v68\n" \
    "v_cvt_pk_bf16_f32 v57, v66, v67\n" \
    "v_mfma_f32_32x32x16_bf16 v[80:95], v[52:55], v[32:35], 0\n" \
    "ds_read_b128 v[52:55], %[ka2] offset:2048\n" \
    "v_exp_f32 v69, v69\n" \
    "v_exp_f32 v70, v70\n" \
    "v_cvt_pk_bf16_f32 v58, v68, v69\n" \
    "v_exp_f32 v71, v71\n" \
    "v_mfma_f32_32x32x16_bf16 v[80:95], v[48:51], v[24:27], v[80:95]\n" \
    "ds_read_b32 v48, %[xa2] offset:256\n" \
    "v_exp_f32 v72, v72\n" \
    "v_cvt_pk_bf16_f32 v59, v70, v71\n" \
    "v_exp_f32 v73, v73\n" \
    "v_exp_f32 v74, v74\n" \
    "s_waitcnt lgkmcnt(3)\n" \
    "v_mfma_f32_32x32x16_bf16 v[96:111], v[44:47], v[56:59], v[96:111]\n" \
    "v_exp_f32 v75, v75\n" \
    "v_cvt_pk_bf16_f32 v60, v72, v73\n" \
    "v_exp_f32 v76, v76\n" \
    "v_exp_f32 v77, v77\n" \
    "v_cvt_pk_bf16_f32 v61, v74, v75\n" \
    "v_exp_f32 v78, v78\n" \
    "v_exp_f32 v79, v79\n" \
    "v_cvt_pk_bf16_f32 v62, v76, v77\n" \
    "s_waitcnt lgkmcnt(2)\n" \
    "v_cvt_pk_bf16_f32 v63, v78, v79\n" \
    "v_exp_f32 v80, v80\n" \
    "v_exp_f32 v81, v81\n" \
    "v_mfma_f32_32x32x16_bf16 v[96:111], v[40:43], v[60:63], v[96:111]\n" \
    "s_waitcnt lgkmcnt(1)\n" \
    "v_exp_f32 v82, v82\n" \
    "v_cvt_pk_bf16_f32 v56, v80, v81\n" \
    "v_exp_f32 v83, v83\n" \
    "v_exp_f32 v84, v84\n" \
    "v_cvt_pk_bf16_f32 v57, v82, v83\n" \
    "v_mfma_f32_32x32x16_bf16 v[64:79], v[52:55], v[36:39], 0\n" \
    "s_waitcnt lgkmcnt(0)\n" \
    "v_exp_f32 v85, v85\n" \
    "v_exp_f32 v86, v86\n" \
    "v_cvt_pk_bf16_f32 v58, v84, v85\n" \
    "v_exp_f32 v87, v87\n" \
    "v_mfma_f32_32x32x16_bf16 v[64:79], v[48:51], v[28:31], v[64:79]\n" \
    "s_mov_b32 m0, %[lp5]\n" \
    "v_exp_f32 v88, v88\n" \
    "buffer_load_dwordx4 %[vo], %[srd], %[gp0] offen sc1 lds\n" \
    "v_cvt_pk_bf16_f32 v59, v86, v87\n" \
    "v_exp_f32 v89, v89\n" \
    "v_exp_f32 v90, v90\n" \
    "v_mfma_f32_32x32x16_bf16 v[112:127], v[44:47], v[56:59], v[112:127]\n" \
    "ds_read_b128 v[44:47], %[va2] offset:2560\n" \
    "v_exp_f32 v91, v91\n" \
    "v_cvt_pk_bf16_f32 v60, v88, v89\n" \
    "v_exp_f32 v92, v92\n" \
    "v_exp_f32 v93, v93\n" \
    "v_cvt_pk_bf16_f32 v61, v90, v91\n" \
    "v_exp_f32 v94, v94\n" \
    "v_exp_f32 v95, v95\n" \
    "v_cvt_pk_bf16_f32 v62, v92, v93\n" \
    "s_nop 0\n" \
    "v_cvt_pk_bf16_f32 v63, v94, v95\n" \
    "v_exp_f32 v64, v64\n" \
    "v_exp_f32 v65, v65\n" \
    "v_mfma_f32_32x32x16_bf16 v[112:127], v[40:43], v[60:63], v[112:127]\n" \
    "ds_read_b128 v[40:43], %[va2] offset:3200\n" \
    "v_exp_f32 v66, v66\n" \
    "v_cvt_pk_bf16_f32 v56, v64, v65\n" \
    "v_exp_f32 v67, v67\n" \
    "v_exp_f32 v68, v68\n" \
    "v_cvt_pk_bf16_f32 v57, v66, v67\n" \
    "v_mfma_f32_32x32x16_bf16 v[80:95], v[52:55], v[32:35], 0\n" \
    "ds_read_b128 v[52:55], %[ka2] offset:3072\n" \
    "v_exp_f32 v69, v69\n" \
    "v_exp_f32 v70, v70\n" \
    "v_cvt_pk_bf16_f32 v58, v68, v69\n" \
    "v_exp_f32 v71, v71\n" \
    "v_mfma_f32_32x32x16_bf16 v[80:95], v[48:51], v[24:27], v[80:95]\n" \
    "ds_read_b32 v48, %[xa2] offset:384\n" \
    "v_exp_f32 v72, v72\n" \
    "v_cvt_pk_bf16_f32 v59, v70, v71\n" \
    "v_exp_f32 v73, v73\n" \
    "v_exp_f32 v74, v74\n" \
    "s_waitcnt lgkmcnt(3)\n" \
    "v_mfma_f32_32x32x16_bf16 v[96:111], v[44:47], v[56:59], v[96:111]\n" \
    "v_exp_f32 v75, v75\n" \
    "v_cvt_pk_bf16_f32 v60, v72, v73\n" \
    "v_exp_f32 v76, v76\n" \
    "v_exp_f32 v77, v77\n" \
    "v_cvt_pk_bf16_f32 v61, v74, v75\n" \
    "v_exp_f32 v78, v78\n" \
    "v_exp_f32 v79, v79\n" \
    "v_cvt_pk_bf16_f32 v62, v76, v77\n" \
    "s_waitcnt lgkmcnt(2)\n" \
    "v_cvt_pk_bf16_f32 v63, v78, v79\n" \
    "v_exp_f32 v80, v80\n" \
    "v_exp_f32 v81, v81\n" \
    "v_mfma_f32_32x32x16_bf16 v[96:111], v[40:43], v[60:63], v[96:111]\n" \
    "s_waitcnt lgkmcnt(1)\n" \
    "v_exp_f32 v82, v82\n" \
    "v_cvt_pk_bf16_f32 v56, v80, v81\n" \
    "v_exp_f32 v83, v83\n" \
    "v_exp_f32 v84, v84\n" \
    "v_cvt_pk_bf16_f32 v57, v82, v83\n" \
    "v_mfma_f32_32x32x16_bf16 v[64:79], v[52:55], v[36:39], 0\n" \
    "s_waitcnt lgkmcnt(0)\n" \
    "v_exp_f32 v85, v85\n" \
    "v_exp_f32 v86, v86\n" \
    "v_cvt_pk_bf16_f32 v58, v84, v85\n" \
    "v_exp_f32 v87, v87\n" \
    "v_mfma_f32_32x32x16_bf16 v[64:79], v[48:51], v[28:31], v[64:79]\n" \
    "s_mov_b32 m0, %[lp6]\n" \
    "v_exp_f32 v88, v88\n" \
    "buffer_load_dwordx4 %[vo], %[srd], %[gp1] offen sc1 lds\n" \
    "v_cvt_pk_bf16_f32 v59, v86, v87\n" \
    "v_exp_f32 v89, v89\n" \
    "v_exp_f32 v90, v90\n" \
    "v_mfma_f32_32x32x16_bf16 v[112:127], v[44:47], v[56:59], v[112:127]\n" \
    "ds_read_b128 v[44:47], %[va2] offset:3840\n" \
    "v_exp_f32 v91, v91\n" \
    "v_cvt_pk_bf16_f32 v60, v88, v89\n" \
    "v_exp_f32 v92, v92\n" \
    "v_exp_f32 v93, v93\n" \
    "v_cvt_pk_bf16_f32 v61, v90, v91\n" \
    "v_exp_f32 v94, v94\n" \
    "v_exp_f32 v95, v95\n" \
    "v_cvt_pk_bf16_f32 v62, v92, v93\n" \
    "s_nop 0\n" \
    "v_cvt_pk_bf16_f32 v63, v94, v95\n" \
    "v_exp_f32 v64, v64\n" \
    "v_exp_f32 v65, v65\n" \
    "v_mfma_f32_32x32x16_bf16 v[112:127], v[40:43], v[60:63], v[112:127]\n" \
    "s_setprio 0\n" \
    "ds_read_b128 v[40:43], %[va2] offset:4480\n" \
    "v_exp_f32 v66, v66\n" \
    "v_cvt_pk_bf16_f32 v56, v64, v65\n" \
    "v_exp_f32 v67, v67\n" \
    "v_exp_f32 v68, v68\n" \
    "v_cvt_pk_bf16_f32 v57, v66, v67\n" \
    "v_mfma_f32_32x32x16_bf16 v[80:95], v[52:55], v[32:35], 0\n" \
    "ds_read_b128 v[52:55], %[ka2] offset:20480\n" \
    "v_exp_f32 v69, v69\n" \
    "v_exp_f32 v70, v70\n" \
    "v_cvt_pk_bf16_f32 v58, v68, v69\n" \
    "v_exp_f32 v71, v71\n" \
    "v_mfma_f32_32x32x16_bf16 v[80:95], v[48:51], v[24:27], v[80:95]\n" \
    "ds_read_b32 v48, %[xa2] offset:20480\n" \
    "v_exp_f32 v72, v72\n" \
    "v_cvt_pk_bf16_f32 v59, v70, v71\n" \
    "v_exp_f32 v73, v73\n" \
    "v_exp_f32 v74, v74\n" \
    "s_waitcnt lgkmcnt(3)\n" \
    "v_mfma_f32_32x32x16_bf16 v[96:111], v[44:47], v[56:59], v[96:111]\n" \
    "v_exp_f32 v75, v75\n" \
    "v_cvt_pk_bf16_f32 v60, v72, v73\n" \
    "v_exp_f32 v76, v76\n" \
    "v_exp_f32 v77, v77\n" \
    "v_cvt_pk_bf16_f32 v61, v74, v75\n" \
    "v_exp_f32 v78, v78\n" \
    "v_exp_f32 v79, v79\n" \
    "v_cvt_pk_bf16_f32 v62, v76, v77\n" \
    "s_waitcnt lgkmcnt(2)\n" \
    "v_cvt_pk_bf16_f32 v63, v78, v79\n" \
    "v_exp_f32 v80, v80\n" \
    "v_exp_f32 v81, v81\n" \
    "v_mfma_f32_32x32x16_bf16 v[96:111], v[40:43], v[60:63], v[96:111]\n" \
    "s_setprio 3\n" \
    "s_waitcnt lgkmcnt(1)\n" \
    "v_exp_f32 v82, v82\n" \
    "v_cvt_pk_bf16_f32 v56, v80, v81\n" \
    "v_exp_f32 v83, v83\n" \
    "v_exp_f32 v84, v84\n" \
    "v_cvt_pk_bf16_f32 v57, v82, v83\n" \
    "v_mfma_f32_32x32x16_bf16 v[64:79], v[52:55], v[36:39], 0\n" \
    "s_waitcnt lgkmcnt(0)\n" \
    "v_exp_f32 v85, v85\n" \
    "v_exp_f32 v86, v86\n" \
    "v_cvt_pk_bf16_f32 v58, v84, v85\n" \
    "v_exp_f32 v87, v87\n" \
    "v_mfma_f32_32x32x16_bf16 v[64:79], v[48:51], v[28:31], v[64:79]\n" \
    "v_exp_f32 v88, v88\n" \
    "v_cvt_pk_bf16_f32 v59, v86, v87\n" \
    "v_exp_f32 v89, v89\n" \
    "v_exp_f32 v90, v90\n" \
    "v_mfma_f32_32x32x16_bf16 v[112:127], v[44:47], v[56:59], v[112:127]\n" \
    "ds_read_b128 v[44:47], %[va2] offset:20480\n" \
    "v_exp_f32 v91, v91\n" \
    "v_cvt_pk_bf16_f32 v60, v88, v89\n" \
    "v_exp_f32 v92, v92\n" \
    "v_exp_f32 v93, v93\n" \
    "v_cvt_pk_bf16_f32 v61, v90, v91\n" \
    "v_exp_f32 v94, v94\n" \
    "v_exp_f32 v95, v95\n" \
    "v_cvt_pk_bf16_f32 v62, v92, v93\n" \
    "s_nop 0\n" \
    "v_cvt_pk_bf16_f32 v63, v94, v95\n" \
    "v_exp_f32 v64, v64\n" \
    "v_exp_f32 v65, v65\n" \
    "v_mfma_f32_32x32x16_bf16 v[112:127], v[40:43], v[60:63], v[112:127]\n" \
    "ds_read_b128 v[40:43], %[va2] offset:21120\n" \
    "v_exp_f32 v66, v66\n" \
    "v_cvt_pk_bf16_f32 v56, v64, v65\n" \
    "v_exp_f32 v67, v67\n" \
    "v_exp_f32 v68, v68\n" \
    "v_cvt_pk_bf16_f32 v57, v66, v67\n" \
    "v_mfma_f32_32x32x16_bf16 v[80:95], v[52:55], v[32:35], 0\n" \
    "ds_read_b128 v[52:55], %[ka2] offset:21504\n" \
    "v_exp_f32 v69, v69\n" \
    "v_exp_f32 v70, v70\n" \
    "v_cvt_pk_bf16_f32 v58, v68, v69\n" \
    "v_exp_f32 v71, v71\n" \
    "v_mfma_f32_32x32x16_bf16 v[80:95], v[48:51], v[24:27], v[80:95]\n" \
    "ds_read_b32 v48, %[xa2] offset:20608\n" \
    "v_exp_f32 v72, v72\n" \
    "v_cvt_pk_bf16_f32 v59, v70, v71\n" \
    "v_exp_f32 v73, v73\n" \
    "v_exp_f32 v74, v74\n" \
    "s_waitcnt lgkmcnt(3)\n" \
    "v_mfma_f32_32x32x16_bf16 v[96:111], v[44:47], v[56:59], v[96:111]\n" \
    "v_exp_f32 v75, v75\n" \
    "v_cvt_pk_bf16_f32 v60, v72, v73\n" \
    "v_exp_f32 v76, v76\n" \
    "v_exp_f32 v77, v77\n" \
    "v_cvt_pk_bf16_f32 v61, v74, v75\n" \
    "v_exp_f32 v78, v78\n" \
    "v_exp_f32 v79, v79\n" \
    "v_cvt_pk_bf16_f32 v62, v76, v77\n" \
    "s_waitcnt lgkmcnt(2)\n" \
    "v_cvt_pk_bf16_f32 v63, v78, v79\n" \
    "v_exp_f32 v80, v80\n" \
    "v_exp_f32 v81, v81\n" \
    "v_mfma_f32_32x32x16_bf16 v[96:111], v[40:43], v[60:63], v[96:111]\n" \
    "s_waitcnt lgkmcnt(1)\n" \
    "v_exp_f32 v82, v82\n" \
    "v_cvt_pk_bf16_f32 v56, v80, v81\n" \
    "v_exp_f32 v83, v83\n" \
    "v_exp_f32 v84, v84\n" \
    "v_cvt_pk_bf16_f32 v57, v82, v83\n" \
    "v_mfma_f32_32x32x16_bf16 v[64:79], v[52:55], v[36:39], 0\n" \
    "s_waitcnt lgkmcnt(0)\n" \
    "v_exp_f32 v85, v85\n" \
    "v_exp_f32 v86, v86\n" \
    "v_cvt_pk_bf16_f32 v58, v84, v85\n" \
    "v_exp_f32 v87, v87\n" \
    "v_mfma_f32_32x32x16_bf16 v[64:79], v[48:51], v[28:31], v[64:79]\n" \
    "v_exp_f32 v88, v88\n" \
    "v_cvt_pk_bf16_f32 v59, v86, v87\n" \
    "v_exp_f32 v89, v89\n" \
    "v_exp_f32 v90, v90\n" \
    "v_mfma_f32_32x32x16_bf16 v[112:127], v[44:47], v[56:59], v[112:127]\n" \
    "ds_read_b128 v[44:47], %[va2] offset:21760\n" \
    "v_exp_f32 v91, v91\n" \
    "v_cvt_pk_bf16_f32 v60, v88, v89\n" \
    "v_exp_f32 v92, v92\n" \
    "v_exp_f32 v93, v93\n" \
    "v_cvt_pk_bf16_f32 v61, v90, v91\n" \
    "v_exp_f32 v94, v94\n" \
    "v_exp_f32 v95, v95\n" \
    "v_cvt_pk_bf16_f32 v62, v92, v93\n" \
    "s_nop 0\n" \
    "v_cvt_pk_bf16_f32 v63, v94, v95\n" \
    "s_waitcnt vmcnt(2)\n" \
    "s_barrier\n" \
    "v_exp_f32 v64, v64\n" \
    "v_exp_f32 v65, v65\n" \
    "v_mfma_f32_32x32x16_bf16 v[112:127], v[40:43], v[60:63], v[112:127]\n" \
    "ds_read_b128 v[40:43], %[va2] offset:22400\n" \
    "v_exp_f32 v66, v66\n" \
    "v_cvt_pk_bf16_f32 v56, v64, v65\n" \
    "v_exp_f32 v67, v67\n" \
    "v_exp_f32 v68, v68\n" \
    "v_cvt_pk_bf16_f32 v57, v66, v67\n" \
    "v_mfma_f32_32x32x16_bf16 v[80:95], v[52:55], v[32:35], 0\n" \
    "ds_read_b128 v[52:55], %[ka2] offset:22528\n" \
    "v_exp_f32 v69, v69\n" \
    "v_exp_f32 v70, v70\n" \
    "v_cvt_pk_bf16_f32 v58, v68, v69\n" \
    "v_exp_f32 v71, v71\n" \
    "v_mfma_f32_32x32x16_bf16 v[80:95], v[48:51], v[24:27], v[80:95]\n" \
    "ds_read_b32 v48, %[xa2] offset:20736\n" \
    "v_exp_f32 v72, v72\n" \
    "v_cvt_pk_bf16_f32 v59, v70, v71\n" \
    "v_exp_f32 v73, v73\n" \
    "v_exp_f32 v74, v74\n" \
    "s_waitcnt lgkmcnt(3)\n" \
    "v_mfma_f32_32x32x16_bf16 v[96:111], v[44:47], v[56:59], v[96:111]\n" \
    "v_exp_f32 v75, v75\n" \
    "v_cvt_pk_bf16_f32 v60, v72, v73\n" \
    "v_exp_f32 v76, v76\n" \
    "v_exp_f32 v77, v77\n" \
    "v_cvt_pk_bf16_f32 v61, v74, v75\n" \
    "v_exp_f32 v78, v78\n" \
    "v_exp_f32 v79, v79\n" \
    "v_cvt_pk_bf16_f32 v62, v76, v77\n" \
    "s_waitcnt lgkmcnt(2)\n" \
    "v_cvt_pk_bf16_f32 v63, v78, v79\n" \
    "v_exp_f32 v80, v80\n" \
    "v_exp_f32 v81, v81\n" \
    "v_mfma_f32_32x32x16_bf16 v[96:111], v[40:43], v[60:63], v[96:111]\n" \
    "s_waitcnt lgkmcnt(1)\n" \
    "v_exp_f32 v82, v82\n" \
    "v_cvt_pk_bf16_f32 v56, v80, v81\n" \
    "v_exp_f32 v83, v83\n" \
    "v_exp_f32 v84, v84\n" \
    "v_cvt_pk_bf16_f32 v57, v82, v83\n" \
    "v_mfma_f32_32x32x16_bf16 v[64:79], v[52:55], v[36:39], 0\n" \
    "s_waitcnt lgkmcnt(0)\n" \
    "v_exp_f32 v85, v85\n" \
    "v_exp_f32 v86, v86\n" \
    "v_cvt_pk_bf16_f32 v58, v84, v85\n" \
    "v_exp_f32 v87, v87\n" \
    "v_mfma_f32_32x32x16_bf16 v[64:79], v[48:51], v[28:31], v[64:79]\n" \
    "s_mov_b32 m0, %[lp7]\n" \
    "v_exp_f32 v88, v88\n" \
    "buffer_load_dwordx4 %[vo], %[srd], %[gp2] offen sc1 lds\n" \
    "v_cvt_pk_bf16_f32 v59, v86, v87\n" \
    "v_exp_f32 v89, v89\n" \
    "v_exp_f32 v90, v90\n" \
    "v_mfma_f32_32x32x16_bf16 v[112:127], v[44:47], v[56:59], v[112:127]\n" \
    "ds_read_b128 v[44:47], %[va2] offset:23040\n" \
    "v_exp_f32 v91, v91\n" \
    "v_cvt_pk_bf16_f32 v60, v88, v89\n" \
    "v_exp_f32 v92, v92\n" \
    "v_exp_f32 v93, v93\n" \
    "v_cvt_pk_bf16_f32 v61, v90, v91\n" \
    "v_exp_f32 v94, v94\n" \
    "v_exp_f32 v95, v95\n" \
    "v_cvt_pk_bf16_f32 v62, v92, v93\n" \
    "s_nop 0\n" \
    "v_cvt_pk_bf16_f32 v63, v94, v95\n" \
    "v_exp_f32 v64, v64\n" \
    "v_exp_f32 v65, v65\n" \
    "v_mfma_f32_32x32x16_bf16 v[112:127], v[40:43], v[60:63], v[112:127]\n" \
    "ds_read_b128 v[40:43], %[va2] offset:23680\n" \
    "v_exp_f32 v66, v66\n" \
    "v_cvt_pk_bf16_f32 v56, v64, v65\n" \
    "v_exp_f32 v67, v67\n" \
    "v_exp_f32 v68, v68\n" \
    "v_cvt_pk_bf16_f32 v57, v66, v67\n" \
    "v_mfma_f32_32x32x16_bf16 v[80:95], v[52:55], v[32:35], 0\n" \
    "ds_read_b128 v[52:55], %[ka2] offset:23552\n" \
    "v_exp_f32 v69, v69\n" \
    "v_exp_f32 v70, v70\n" \
    "v_cvt_pk_bf16_f32 v58, v68, v69\n" \
    "v_exp_f32 v71, v71\n" \
    "v_mfma_f32_32x32x16_bf16 v[80:95], v[48:51], v[24:27], v[80:95]\n" \
    "ds_read_b32 v48, %[xa2] offset:20864\n" \
    "v_exp_f32 v72, v72\n" \
    "v_cvt_pk_bf16_f32 v59, v70, v71\n" \
    "v_exp_f32 v73, v73\n" \
    "v_exp_f32 v74, v74\n" \
    "s_waitcnt lgkmcnt(3)\n" \
    "v_mfma_f32_32x32x16_bf16 v[96:111], v[44:47], v[56:59], v[96:111]\n" \
    "v_exp_f32 v75, v75\n" \
    "v_cvt_pk_bf16_f32 v60, v72, v73\n" \
    "v_exp_f32 v76, v76\n" \
    "v_exp_f32 v77, v77\n" \
    "v_cvt_pk_bf16_f32 v61, v74, v75\n" \
    "v_exp_f32 v78, v78\n" \
    "v_exp_f32 v79, v79\n" \
    "v_cvt_pk_bf16_f32 v62, v76, v77\n" \
    "s_waitcnt lgkmcnt(2)\n" \
    "v_cvt_pk_bf16_f32 v63, v78, v79\n" \
    "v_exp_f32 v80, v80\n" \
    "v_exp_f32 v81, v81\n" \
    "v_mfma_f32_32x32x16_bf16 v[96:111], v[40:43], v[60:63], v[96:111]\n" \
    "s_waitcnt lgkmcnt(1)\n" \
    "v_exp_f32 v82, v82\n" \
    "v_cvt_pk_bf16_f32 v56, v80, v81\n" \
    "v_exp_f32 v83, v83\n" \
    "v_exp_f32 v84, v84\n" \
    "v_cvt_pk_bf16_f32 v57, v82, v83\n" \
    "v_mfma_f32_32x32x16_bf16 v[64:79], v[52:55], v[36:39], 0\n" \
    "s_waitcnt lgkmcnt(0)\n" \
    "v_exp_f32 v85, v85\n" \
    "v_exp_f32 v86, v86\n" \
    "v_cvt_pk_bf16_f32 v58, v84, v85\n" \
    "v_exp_f32 v87, v87\n" \
    "v_mfma_f32_32x32x16_bf16 v[64:79], v[48:51], v[28:31], v[64:79]\n" \
    "s_mov_b32 m0, %[lp8]\n" \
    "v_exp_f32 v88, v88\n" \
    "buffer_load_dwordx4 %[vo], %[srd], %[gp3] offen sc1 lds\n" \
    "v_cvt_pk_bf16_f32 v59, v86, v87\n" \
    "v_exp_f32 v89, v89\n" \
    "v_exp_f32 v90, v90\n" \
    "v_mfma_f32_32x32x16_bf16 v[112:127], v[44:47], v[56:59], v[112:127]\n" \
    "ds_read_b128 v[44:47], %[va2] offset:24320\n" \
    "v_exp_f32 v91, v91\n" \
    "v_cvt_pk_bf16_f32 v60, v88, v89\n" \
    "v_exp_f32 v92, v92\n" \
    "v_exp_f32 v93, v93\n" \
    "v_cvt_pk_bf16_f32 v61, v90, v91\n" \
    "v_exp_f32 v94, v94\n" \
    "v_exp_f32 v95, v95\n" \
    "v_cvt_pk_bf16_f32 v62, v92, v93\n" \
    "s_nop 0\n" \
    "v_cvt_pk_bf16_f32 v63, v94, v95\n" \
    "v_exp_f32 v64, v64\n" \
    "v_exp_f32 v65, v65\n" \
    "v_mfma_f32_32x32x16_bf16 v[112:127], v[40:43], v[60:63], v[112:127]\n" \
    "s_setprio 0\n" \
    "ds_read_b128 v[40:43], %[va2] offset:24960\n" \
    "v_exp_f32 v66, v66\n" \
    "v_cvt_pk_bf16_f32 v56, v64, v65\n" \
    "v_exp_f32 v67, v67\n" \
    "v_exp_f32 v68, v68\n" \
    "v_cvt_pk_bf16_f32 v57, v66, v67\n" \
    "v_mfma_f32_32x32x16_bf16 v[80:95], v[52:55], v[32:35], 0\n" \
    "ds_read_b128 v[52:55], %[ka] offset:0\n" \
    "v_exp_f32 v69, v69\n" \
    "v_exp_f32 v70, v70\n" \
    "v_cvt_pk_bf16_f32 v58, v68, v69\n" \
    "v_exp_f32 v71, v71\n" \
    "v_mfma_f32_32x32x16_bf16 v[80:95], v[48:51], v[24:27], v[80:95]\n" \
    "ds_read_b32 v48, %[xa] offset:0\n" \
    "v_exp_f32 v72, v72\n" \
    "v_cvt_pk_bf16_f32 v59, v70, v71\n" \
    "v_exp_f32 v73, v73\n" \
    "v_exp_f32 v74, v74\n" \
    "s_waitcnt lgkmcnt(3)\n" \
    "v_mfma_f32_32x32x16_bf16 v[96:111], v[44:47], v[56:59], v[96:111]\n" \
    "v_exp_f32 v75, v75\n" \
    "v_cvt_pk_bf16_f32 v60, v72, v73\n" \
    "v_exp_f32 v76, v76\n" \
    "v_exp_f32 v77, v77\n" \
    "v_cvt_pk_bf16_f32 v61, v74, v75\n" \
    "v_exp_f32 v78, v78\n" \
    "v_exp_f32 v79, v79\n" \
    "v_cvt_pk_bf16_f32 v62, v76, v77\n" \
    "s_waitcnt lgkmcnt(2)\n" \
    "v_cvt_pk_bf16_f32 v63, v78, v79\n" \
    "v_exp_f32 v80, v80\n" \
    "v_exp_f32 v81, v81\n" \
    "v_mfma_f32_32x32x16_bf16 v[96:111], v[40:43], v[60:63], v[96:111]\n" \
    "s_setprio 3\n" \
    "s_waitcnt lgkmcnt(1)\n" \
    "v_exp_f32 v82, v82\n" \
    "v_cvt_pk_bf16_f32 v56, v80, v81\n" \
    "v_exp_f32 v83, v83\n" \
    "v_exp_f32 v84, v84\n" \
    "v_cvt_pk_bf16_f32 v57, v82, v83\n" \
    "v_mfma_f32_32x32x16_bf16 v[64:79], v[52:55], v[36:39], 0\n" \
    "s_waitcnt lgkmcnt(0)\n" \
    "v_exp_f32 v85, v85\n" \
    "v_exp_f32 v86, v86\n" \
    "v_cvt_pk_bf16_f32 v58, v84, v85\n" \
    "v_exp_f32 v87, v87\n" \
    "v_mfma_f32_32x32x16_bf16 v[64:79], v[48:51], v[28:31], v[64:79]\n" \
    "s_mov_b32 m0, %[lp9]\n" \
    "v_exp_f32 v88, v88\n" \
    "buffer_load_dwordx4 %[vo], %[srd], %[gp4] offen sc1 lds\n" \
    "v_cvt_pk_bf16_f32 v59, v86, v87\n" \
    "v_exp_f32 v89, v89\n" \
    "v_exp_f32 v90, v90\n" \
    "v_mfma_f32_32x32x16_bf16 v[112:127], v[44:47], v[56:59], v[112:127]\n" \
    "ds_read_b128 v[44:47], %[va] offset:0\n" \
    "v_exp_f32 v91, v91\n" \
    "v_cvt_pk_bf16_f32 v60, v88, v89\n" \
    "v_exp_f32 v92, v92\n" \
    "v_exp_f32 v93, v93\n" \
    "v_cvt_pk_bf16_f32 v61, v90, v91\n" \
    "v_exp_f32 v94, v94\n" \
    "v_exp_f32 v95, v95\n" \
    "v_cvt_pk_bf16_f32 v62, v92, v93\n" \
    "s_nop 0\n" \
    "v_cvt_pk_bf16_f32 v63, v94, v95\n" \
    "v_add_u32 %[vo], 0x4c00, %[vo]\n" \
    "s_add_u32 %[cnt], %[cnt], 1\n" \
    "s_cmp_lt_u32 %[cnt], %[nit]\n" \
    "s_cbranch_scc1 2b\n" \
    ""
#define AST(i)
#define STA(i)
__device__ __forceinline__ f32x16 attn_fallback(const char* kvh, u32x4 q0, unsigned qx, int lane) {
    const int r = lane & 31, hh = lane >> 5;
    const u32x4 q1z = {qx, 0u, 0u, 0u};
    float m = -INFINITY;
#pragma unroll 1
    for (int t = 0; t < 64; t++) {
        const char* ch = kvh + (size_t)(t >> 2) * CHUNK; const int tt = t & 3;
        u32x4 k0 = *(const u32x4*)(ch + tt * 1024 + lane * 16);
        u32x4 kx = {*(const unsigned*)(ch + 4096 + tt * 128 + r * 4), 0u, 0u, 0u};
        f32x16 s = {}; s = MFMA32(k0, q0, s); s = MFMA32(kx, q1z, s);
#pragma unroll
        for (int i = 0; i < 16; i++) m = fmaxf(m, s[i]);
    }
    m = fmaxf(m, __shfl_xor(m, 32));
    f32x16 o = {};
#pragma unroll 1
    for (int t = 0; t < 64; t++) {
        const char* ch = kvh + (size_t)(t >> 2) * CHUNK; const int tt = t & 3;
        u32x4 k0 = *(const u32x4*)(ch + tt * 1024 + lane * 16);
        u32x4 kx = {*(const unsigned*)(ch + 4096 + tt * 128 + r * 4), 0u, 0u, 0u};
        f32x16 s = {}; s = MFMA32(k0, q0, s); s = MFMA32(kx, q1z, s);
        const char* vp = ch + 4608 + tt * 1280 + (hh * 20 + (r < 19 ? r : 19)) * 16;
        u32x4 v0 = *(const u32x4*)vp, v1 = *(const u32x4*)(vp + 640);
        u32x4 p0, p1;
#pragma unroll
        for (int i = 0; i < 4; i++) {
            p0[i] = cvtpk(__builtin_amdgcn_exp2f(s[2 * i] - m), __builtin_amdgcn_exp2f(s[2 * i + 1] - m));
            p1[i] = cvtpk(__builtin_amdgcn_exp2f(s[8 + 2 * i] - m), __builtin_amdgcn_exp2f(s[8 + 2 * i + 1] - m));
        }
        o = MFMA32(v0, p0, o); o = MFMA32(v1, p1, o);
    }
    return o;
}

__device__ __forceinline__ void attn_role(char* ws, char* lds, int h, int qb  , unsigned tag) {
    STA(0);
    const int tid = threadIdx.x, lane = tid & 63, r = lane & 31, hh = lane >> 5;
    const int w = rfl(tid >> 6), qg = w & 3, kh = w >> 2;
    const int qt = qb * 8 + qg * 2;
    const char* kvh = ws + OFF_KV + (size_t)h * 16 * CHUNK;
    const u32x4 srd = make_srd(kvh, 16 * CHUNK);
    const unsigned lds0 = rfl((unsigned)(uintptr_t)lds);
    unsigned vo = lane * 16;
    auto pro = [&](int st) {
#pragma unroll
        for (int k = 0; k < 3; k++) { int p = w + 8 * k; if (p > 19) p = 19; const int u = p / 10, q = p % 10;
            dma16h(srd, vo, (unsigned)((st + 8 * u) * CHUNK + q * 1024), lds0 + st * 20480 + u * 10240 + q * 1024); } };
    pro(0);
    u32x4 q0a, q0b; unsigned qxa, qxb;
    {
        const char* qp = ws + OFF_QF + ((size_t)(h * 64 + qt) * 64 + lane) * 16;
        const char* xp = ws + OFF_QX + ((size_t)h * 2048 + qt * 32 + r) * 4;
        asm volatile("global_load_dwordx4 %0, %4, off sc1\n\tglobal_load_dwordx4 %1, %4, off offset:1024 sc1\n\tglobal_load_dword %2, %5, off sc1\n\tglobal_load_dword %3, %5, off offset:128 sc1"
                     : "=&v"(q0a), "=&v"(q0b), "=&v"(qxa), "=&v"(qxb) : "v"(qp), "v"(xp) : "memory");
    }
    unsigned ka = lds0 + kh * 10240 + lane * 16, xa = lds0 + kh * 10240 + 4096 + r * 4, va = lds0 + kh * 10240 + 4608 + (hh * 20 + (r < 19 ? r : 19)) * 16;
    unsigned ka2 = ka + 40960, xa2 = xa + 40960, va2 = va + 40960;
    unsigned lp[10], gp[5], l1[6], g1[6];
#pragma unroll
    for (int e = 0; e < 5; e++) {
        const int n = w + 8 * e, d = n >= 20 ? 1 : 0, p = n - 20 * d, u = p >= 10 ? 1 : 0, q = p - 10 * u;
        gp[e] = (unsigned)((3 + d + 8 * u) * CHUNK + q * 1024);
        lp[e] = lds0 + ((3 + d) & 3) * 20480 + u * 10240 + q * 1024;
        lp[5 + e] = lds0 + ((5 + d) & 3) * 20480 + u * 10240 + q * 1024;
    }
#pragma unroll
    for (int k = 0; k < 3; k++) { int p = w + 8 * k; if (p > 19) p = 19; const int u = p >= 10 ? 1 : 0, q = p - 10 * u;
        g1[k] = (unsigned)((1 + 8 * u) * CHUNK + q * 1024); l1[k] = lds0 + 20480 + u * 10240 + q * 1024; g1[3 + k] = g1[k] + CHUNK; l1[3 + k] = l1[k] + 20480; }
    asm volatile("" : "+s"(lp[0]), "+s"(lp[1]), "+s"(lp[2]), "+s"(lp[3]), "+s"(lp[4]), "+s"(lp[5]), "+s"(lp[6]), "+s"(lp[7]), "+s"(lp[8]), "+s"(lp[9]),
                      "+s"(gp[0]), "+s"(gp[1]), "+s"(gp[2]), "+s"(gp[3]), "+s"(gp[4]));
    asm volatile("" : "+s"(l1[0]), "+s"(l1[1]), "+s"(l1[2]), "+s"(l1[3]), "+s"(l1[4]), "+s"(l1[5]), "+s"(g1[0]), "+s"(g1[1]), "+s"(g1[2]), "+s"(g1[3]), "+s"(g1[4]), "+s"(g1[5]),
                      "+v"(ka), "+v"(xa), "+v"(va), "+v"(ka2), "+v"(xa2), "+v"(va2));
    asm volatile("s_waitcnt vmcnt(0)\n\ts_barrier" : "+v"(q0a), "+v"(q0b), "+v"(qxa), "+v"(qxb) :: "memory");
    AST(2);
#pragma unroll
    for (int k = 0; k < 6; k++) dma16h(srd, vo, g1[k], l1[k]);
    u32x4 q1a = {hh ? 0u : qxa, 0u, 0u, 0u}, q1b = {hh ? 0u : qxb, 0u, 0u, 0u};
    unsigned cnt = 0, keep; const unsigned nit = 2;
    const unsigned long long lomask = 0xffffffffull;
    float nma, nmb;
    f32x16 oa, ob;
    asm volatile(
        "s_mov_b32 %[keep], m0\n"
        "ds_read_b128 v[52:55], %[ka]\n ds_read_b32 v48, %[xa]\n ds_read_b128 v[44:47], %[va]\n"
        "v_mov_b32 v49, 0x3f80\n v_mov_b32 v50, 0\n v_mov_b32 v51, 0\n"
        "s_waitcnt lgkmcnt(1)\n s_nop 0\n"
        "v_mfma_f32_32x32x16_bf16 v[64:79], v[52:55], v[36:39], 0\n"
        "v_mfma_f32_32x32x16_bf16 v[80:95], v[52:55], v[32:35], 0\n"
        "v_mfma_f32_32x32x16_bf16 v[64:79], v[48:51], v[28:31], v[64:79]\n"
        "v_mfma_f32_32x32x16_bf16 v[80:95], v[48:51], v[24:27], v[80:95]\n"
        "v_mov_b32 v60, 0\n v_mov_b32 v61, 0\n v_mov_b32 v62, 0\n v_mov_b32 v63, 0\n"
        "v_mov_b32 v40, 0\n v_mov_b32 v41, 0\n v_mov_b32 v42, 0\n v_mov_b32 v43, 0\n"
        "v_mov_b32 v96, 0\n v_mov_b32 v97, 0\n v_mov_b32 v98, 0\n v_mov_b32 v99, 0\n v_mov_b32 v100, 0\n v_mov_b32 v101, 0\n v_mov_b32 v102, 0\n v_mov_b32 v103, 0\n"
        "v_mov_b32 v104, 0\n v_mov_b32 v105, 0\n v_mov_b32 v106, 0\n v_mov_b32 v107, 0\n v_mov_b32 v108, 0\n v_mov_b32 v109, 0\n v_mov_b32 v110, 0\n v_mov_b32 v111, 0\n"
        "v_mov_b32 v112, 0\n v_mov_b32 v113, 0\n v_mov_b32 v114, 0\n v_mov_b32 v115, 0\n v_mov_b32 v116, 0\n v_mov_b32 v117, 0\n v_mov_b32 v118, 0\n v_mov_b32 v119, 0\n"
        "v_mov_b32 v120, 0\n v_mov_b32 v121, 0\n v_mov_b32 v122, 0\n v_mov_b32 v123, 0\n v_mov_b32 v124, 0\n v_mov_b32 v125, 0\n v_mov_b32 v126, 0\n v_mov_b32 v127, 0\n"
        "v_max3_f32 v56, v64, v65, v66\n v_max3_f32 v57, v67, v68, v69\n v_max3_f32 v58, v70, v71, v72\n v_max3_f32 v59, v73, v74, v75\n"
        "v_max3_f32 v56, v56, v76, v77\n v_max3_f32 v57, v57, v78, v79\n v_max3_f32 v56, v56, v58, v59\n v_max_f32 v56, v56, v57\n"
        "v_max3_f32 v58, v80, v81, v82\n v_max3_f32 v59, v83, v84, v85\n v_max3_f32 v57, v86, v87, v88\n v_max3_f32 v58, v58, v89, v90\n"
        "v_max3_f32 v59, v59, v91, v92\n v_max3_f32 v57, v57, v93, v94\n v_max3_f32 v58, v58, v59, v95\n v_max_f32 v58, v58, v57\n"
        "v_mov_b32 v57, v56\n v_mov_b32 v59, v58\n s_nop 1\n"
        "v_permlane32_swap_b32 v56, v57\n v_permlane32_swap_b32 v58, v59\n s_nop 1\n"
        "v_max_f32 v56, v56, v57\n v_max_f32 v58, v58, v59\n"
        "v_cvt_pk_bf16_f32 v56, -v56, 0\n v_cvt_pk_bf16_f32 v58, -v58, 0\n s_nop 0\n"
        "v_cndmask_b32 v29, 0, v56, %[lom]\n v_cndmask_b32 v25, 0, v58, %[lom]\n"
        "v_lshlrev_b32 %[nma], 16, v56\n v_lshlrev_b32 %[nmb], 16, v58\n"
        "v_mfma_f32_32x32x16_bf16 v[64:79], v[52:55], v[36:39], 0\n"
        "v_mfma_f32_32x32x16_bf16 v[64:79], v[48:51], v[28:31], v[64:79]\n"
        "s_waitcnt lgkmcnt(0)\n s_nop 7\n s_nop 7\n"
        ATT_LOOP_ASM
        "s_nop 1\n"
        "v_mfma_f32_32x32x16_bf16 v[112:127], v[40:43], v[60:63], v[112:127]\n"
        "s_waitcnt vmcnt(0) lgkmcnt(0)\n s_barrier\n"
        "s_nop 7\n s_nop 7\n"
        "s_mov_b32 m0, %[keep]\n"
        : "={v[96:111]}"(oa), "={v[112:127]}"(ob), "+{v[28:31]}"(q1a), "+{v[24:27]}"(q1b), [nma] "=&v"(nma), [nmb] "=&v"(nmb), [vo] "+v"(vo), [cnt] "+s"(cnt), [keep] "=&s"(keep)
        : "{v[36:39]}"(q0a), "{v[32:35]}"(q0b), [lom] "s"(lomask),
          [ka] "v"(ka), [xa] "v"(xa), [va] "v"(va), [ka2] "v"(ka2), [xa2] "v"(xa2), [va2] "v"(va2),
          [lp0] "s"(lp[0]), [lp1] "s"(lp[1]), [lp2] "s"(lp[2]), [lp3] "s"(lp[3]), [lp4] "s"(lp[4]),
          [lp5] "s"(lp[5]), [lp6] "s"(lp[6]), [lp7] "s"(lp[7]), [lp8] "s"(lp[8]), [lp9] "s"(lp[9]),
          [gp0] "s"(gp[0]), [gp1] "s"(gp[1]), [gp2] "s"(gp[2]), [gp3] "s"(gp[3]), [gp4] "s"(gp[4]), [srd] "s"(srd), [nit] "s"(nit)
        : "memory", "scc", "v40", "v41", "v42", "v43", "v44", "v45", "v46", "v47", "v48", "v49", "v50", "v51", "v52", "v53", "v54", "v55",
          "v56", "v57", "v58", "v59", "v60", "v61", "v62", "v63",
          "v64", "v65", "v66", "v67", "v68", "v69", "v70", "v71", "v72", "v73", "v74", "v75", "v76", "v77", "v78", "v79",
          "v80", "v81", "v82", "v83", "v84", "v85", "v86", "v87", "v88", "v89", "v90", "v91", "v92", "v93", "v94", "v95");
    AST(3);
    int tid2 = threadIdx.x; asm volatile("" : "+v"(tid2));
    const int lane2 = tid2 & 63, r2 = lane2 & 31, hh2 = lane2 >> 5;
    const bool bad_a = __any(hh2 == 0 && !(oa[10] < 1.152921504606846976e18f && oa[10] > 0.25f));
    const bool bad_b = __any(hh2 == 0 && !(ob[10] < 1.152921504606846976e18f && ob[10] > 0.25f));
    f32x16 mine = kh ? ob : oa; const f32x16 give = kh ? oa : ob;
    const float nm = kh ? nmb : nma; const float ng = (kh ? bad_a : bad_b) ? __builtin_nanf("") : (kh ? nma : nmb);
    bool bad = kh ? bad_b : bad_a;
    {
        typedef __attribute__((address_space(3))) f32x4* lf4;
        lf4 wp = (lf4)((lds_ptr)lds + w * 3072 + lane2 * 16);
        wp[0] = f32x4{give[0], give[1], give[2], give[3]}; wp[64] = f32x4{give[4], give[5], give[6], give[7]}; wp[128] = f32x4{give[8], give[9], give[10], ng};
        asm volatile("s_waitcnt lgkmcnt(0)\n\ts_barrier" ::: "memory");
        lf4 rp = (lf4)((lds_ptr)lds + (w ^ 4) * 3072 + lane2 * 16);
        const f32x4 g0 = rp[0], g1v = rp[64], g2 = rp[128];
        const float no = g2[3];
        bad = bad || __any(no != no);
        const float mn = fminf(nm, no), f0 = __builtin_amdgcn_exp2f(mn - nm), f1 = __builtin_amdgcn_exp2f(mn - no);
#pragma unroll
        for (int i = 0; i < 4; i++) { mine[i] = mine[i] * f0 + g0[i] * f1; mine[4 + i] = mine[4 + i] * f0 + g1v[i] * f1; }
#pragma unroll
        for (int i = 0; i < 3; i++) mine[8 + i] = mine[8 + i] * f0 + g2[i] * f1;
    }
    if (bad) { __builtin_amdgcn_fence(__ATOMIC_ACQUIRE, "agent"); asm volatile("s_waitcnt vmcnt(0)" ::: "memory"); }
    if (bad) mine = attn_fallback(kvh, kh ? q0b : q0a, kh ? q1b[0] : q1a[0], lane2);
    const auto lsw = __builtin_amdgcn_permlane32_swap(__float_as_uint(mine[10]), __float_as_uint(mine[10]), false, false);
    const float inv = __builtin_amdgcn_rcpf(__uint_as_float(lsw[0]));
    const u32x4 ov = {cvtpk(mine[0] * inv, mine[1] * inv), cvtpk(mine[2] * inv, mine[3] * inv), cvtpk(mine[4] * inv, mine[5] * inv), cvtpk(mine[6] * inv, mine[7] * inv)};
    const unsigned ob0 = (unsigned)(OFF_O + ((qt + kh) * 2 + (r2 >> 4)) * 18432 + (r2 & 15) * 16);
    const __amdgpu_buffer_rsrc_t rw = ws_rsrc(ws, WS_USED);
    __builtin_amdgcn_raw_buffer_store_b128(ov, rw, ob0 + (h >> 1) * 1024 + (2 * (h & 1) + hh2) * 256, 0, 16);
    if (!hh2) __builtin_amdgcn_raw_buffer_store_b32(cvtpk(mine[8] * inv, mine[9] * inv), rw, ob0 + (16 + (h >> 4)) * 1024 + ((h & 15) >> 2) * 256 + (h & 3) * 4, 0, 16);
    AST(6);
    drain_bar();
    if (threadIdx.x == 0) publish(ws, FL_ATT + qb * 128 + h * 4, tag);
}
#define QST(i)
#include <type_traits>
#define STQ(i)
template <int N> __device__ __forceinline__ void wait_vm_bar() { asm volatile("s_waitcnt vmcnt(%0)\n\ts_barrier" :: "n"(N) : "memory"); }
__device__ __forceinline__ int swz4(int q) { return (0x78 >> (2 * q)) & 3; }

template <int VMASK>
__device__ __forceinline__ void qkv_role(const u16* x, const u16* wq, char* ws, char* lds, int mt, int nt, int krot, unsigned touch, unsigned tx1, unsigned tx2, unsigned tx3, unsigned& tag) {
    STQ(0);
    const int tid = threadIdx.x, lane = tid & 63, w = rfl(tid >> 6);
    const u32x4 srdx = make_srd(x, 2048 * 576 * 2), srdw = make_srd(wq, 576 * 1728 * 2);
    const unsigned lds0 = rfl((unsigned)(uintptr_t)lds);
    const int i = lane & 15, g = lane >> 4;
    const unsigned vox = (unsigned)((mt * 128 + w * 16 + (lane >> 2)) * 1152 + (((lane & 3) ^ swz4(g)) << 4));
    unsigned vow; bool wuse;
    { const int k = 4 * w + g, sg = (k & 3) | (((k >> 3) & 1) << 2); int cc = i ^ (2 * sg); wuse = cc <= 13; if (cc > 13) cc = 13; vow = (unsigned)(k * 3456 + nt * 216 + cc * 16); }
    auto issue = [&](int s) {
        int ks = s + krot; if (ks >= 18) ks -= 18;
        const unsigned slot = lds0 + (s % 5) * 16384 + w * 1024;
        dma16(srdx, vox, (unsigned)ks * 64u, slot);
        dma16(srdw, vow, (unsigned)ks * (32u * 3456u), slot + 8192); };
    STQ(7);
    issue(0);
    STQ(2);
#pragma unroll
    for (int s = 1; s < 5; s++) issue(s);
    if (w == 0) tag = read_tag(ws);
    STQ(5);
    const int tg = w & 3, cg = w >> 2;
    const lds_ptr xa = (lds_ptr)lds + tg * 2048 + i * 64 + ((g ^ swz4(i >> 2)) << 4);
    lds_ptr wa[4];
    { const int q = i >> 2, p = i & 3, k = 8 * g + q, sg = (k & 3) | (((k >> 3) & 1) << 2);
#pragma unroll
      for (int c = 0; c < 4; c++) {
          const int ct = cg * 4 + c;
          const int col = ct < 6 ? 18 * ct + ((ct & 1) ? 2 : 0) + 4 * p : 36 * (p < 3 ? p : 2) + 16, b = 2 * col;
          wa[c] = (lds_ptr)lds + 8192 + k * 256 + ((((b >> 4) ^ (2 * sg)) << 4) | (b & 8));
      } }
    f32x4 acc[4][2];
#pragma unroll
    for (int c = 0; c < 4; c++) { acc[c][0] = f32x4{0, 0, 0, 0}; acc[c][1] = f32x4{0, 0, 0, 0}; }
    auto mainloop = [&](auto CGc) {
        constexpr int CG = decltype(CGc)::value, NC = CG ? 3 : 4;
#pragma unroll
        for (int m = 0; m < 9; m++) {
            if (m == 0) { asm volatile("s_waitcnt vmcnt(6)\n\ts_barrier" : "+v"(touch), "+v"(tx1), "+v"(tx2), "+v"(tx3) :: "memory"); } else if (m < 8) wait_vm_bar<2>(); else wait_vm_bar<0>();
            if (m == 0) QST(2);
            if (m >= 1) { if (2 * m + 3 < 18) issue(2 * m + 3); if (2 * m + 4 < 18) issue(2 * m + 4); }
            u32x4 xf[2][2], wf[2][4];
#pragma unroll
            for (int z = 0; z < 2; z++) {
                const int off = ((2 * m + z) % 5) * 16384;
                xf[z][0] = lds_r128(xa + off); xf[z][1] = lds_r128(xa + off + 1024);
#pragma unroll
                for (int c = 0; c < NC; c++) { const u32x2 lo = lds_tr(wa[c] + off), hi = lds_tr(wa[c] + off + 1024); wf[z][c] = u32x4{lo[0], lo[1], hi[0], hi[1]}; }
                if (z == 0) __builtin_amdgcn_sched_barrier(0);
            }
#pragma unroll
            for (int z = 0; z < 2; z++)
#pragma unroll
                for (int c = 0; c < NC; c++) {
                    const int ct = CG * 4 + c;
#pragma unroll
                    for (int t = 0; t < 2; t++) {
                        if ((ct < 6 && ((VMASK >> (ct >> 1)) & 1)) || (ct == 6 && VMASK == 7)) acc[c][t] = MFMA16(xf[z][t], wf[z][c], acc[c][t]); else acc[c][t] = MFMA16(wf[z][c], xf[z][t], acc[c][t]);
                        if (ct == 6 && VMASK == 4) acc[3][t] = MFMA16(xf[z][t], wf[z][c], acc[3][t]);
                    }
                }
#pragma unroll
            for (int j = 0; j < 5; j++) { __builtin_amdgcn_sched_group_barrier(0x100, 2, 0); __builtin_amdgcn_sched_group_barrier(0x008, 1, 0); }
            __builtin_amdgcn_sched_barrier(0);
        }
    };
    if (cg == 0) mainloop(std::integral_constant<int, 0>{}); else mainloop(std::integral_constant<int, 1>{});
    QST(3);
    const __amdgpu_buffer_rsrc_t rw = ws_rsrc(ws, WS_USED);
    const float qs = 0.23570226039551584f * 1.4426950408889634f;
    const unsigned vqk = (unsigned)((g >> 1) * 512 + ((g & 1) * 16 + i) * 16);
    const unsigned vv = (unsigned)((g >> 1) * 640 + (g & 1) * 320 + i * 16);
#pragma unroll
    for (int c = 0; c < 4; c++) {
        const int ct = cg * 4 + c;
        if (ct < 6) {
            const int hs = nt * 6 + ct, h = hs & 31;
            const unsigned kv = (unsigned)(OFF_KV + (h * 16 + mt) * CHUNK);
            const bool isv = (VMASK >> (ct >> 1)) & 1, isq = hs < 32;
            const float sc = isq ? qs : 1.0f;
            const unsigned so = isv ? kv + 4608 + tg * 1280 : isq ? (unsigned)(OFF_QF + (h * 64 + mt * 4 + tg) * 1024) : kv + tg * 1024;
            const f32x4 a = acc[c][0], b = acc[c][1];
            const unsigned a0 = cvtpk(a[0] * sc, a[1] * sc), a1 = cvtpk(a[2] * sc, a[3] * sc), b0 = cvtpk(b[0] * sc, b[1] * sc), b1 = cvtpk(b[2] * sc, b[3] * sc);
            if (isv) {
                const auto p0 = __builtin_amdgcn_permlane32_swap(a0, b0, false, false), p1 = __builtin_amdgcn_permlane32_swap(a1, b1, false, false);
                __builtin_amdgcn_raw_buffer_store_b128(u32x4{p0[0], p1[0], p0[1], p1[1]}, rw, vv, so, 16);
            } else {
                const auto p0 = __builtin_amdgcn_permlane16_swap(a0, b0, false, false), p1 = __builtin_amdgcn_permlane16_swap(a1, b1, false, false);
                __builtin_amdgcn_raw_buffer_store_b128(u32x4{p0[0], p1[0], p0[1], p1[1]}, rw, vqk, so, 16);
            }
        }
    }
    if (cg == 1) {
        if (VMASK != 7) {
            const bool isq0 = nt * 6 + 2 * g < 32, isq1 = nt * 6 + 2 * g + 1 < 32;
            const float s0 = isq0 ? qs : 1.0f, s1 = isq1 ? qs : 1.0f;
            unsigned r0 = cvtpk(acc[2][0][0] * s0, acc[2][0][1] * s0), r1 = cvtpk(acc[2][0][2] * s1, acc[2][0][3] * s1);
            unsigned r2 = cvtpk(acc[2][1][0] * s0, acc[2][1][1] * s0), r3 = cvtpk(acc[2][1][2] * s1, acc[2][1][3] * s1);
            const bool o1 = lane & 1, o2 = lane & 2;
            { unsigned t0 = o1 ? r0 : r1, t1 = o1 ? r2 : r3;
              t0 = __builtin_amdgcn_mov_dpp(t0, 0xB1, 0xF, 0xF, true); t1 = __builtin_amdgcn_mov_dpp(t1, 0xB1, 0xF, 0xF, true);
              if (o1) { r0 = t0; r2 = t1; } else { r1 = t0; r3 = t1; } }
            { unsigned t0 = o2 ? r0 : r2, t1 = o2 ? r1 : r3;
              t0 = __builtin_amdgcn_mov_dpp(t0, 0x4E, 0xF, 0xF, true); t1 = __builtin_amdgcn_mov_dpp(t1, 0x4E, 0xF, 0xF, true);
              if (o2) { r0 = t0; r1 = t1; } else { r2 = t0; r3 = t1; } }
            const int j = lane & 3, hs = nt * 6 + 2 * g + (j & 1), h = hs & 31, tl = tg * 32 + (j >> 1) * 16 + (i & 12);
            const unsigned o = hs < 32 ? (unsigned)(OFF_QX + (h * 2048 + mt * 128 + tl) * 4) : (unsigned)(OFF_KV + (h * 16 + mt) * CHUNK) + 4096 + tl * 4;
            if (g < 3 && !((VMASK >> g) & 1)) __builtin_amdgcn_raw_buffer_store_b128(u32x4{r0, r1, r2, r3}, rw, o, 0, 16);
        }
        if (VMASK) {
            const f32x4 a = acc[VMASK == 7 ? 2 : 3][0], b = acc[VMASK == 7 ? 2 : 3][1];
            const unsigned a0 = cvtpk(a[0], a[1]), a1 = cvtpk(a[2], a[3]), b0 = cvtpk(b[0], b[1]), b1 = cvtpk(b[2], b[3]);
            const auto p0 = __builtin_amdgcn_permlane32_swap(a0, b0, false, false), p1 = __builtin_amdgcn_permlane32_swap(a1, b1, false, false);
            const int p = i >> 2, h = (nt * 6 + 2 * p + ((i >> 1) & 1)) & 31;
            if (p < 3 && ((VMASK >> p) & 1))
                __builtin_amdgcn_raw_buffer_store_b128(u32x4{p0[0], p1[0], p0[1], p1[1]}, rw, (unsigned)(OFF_KV + (h * 16 + mt) * CHUNK) + 4608 + tg * 1280 + (g >> 1) * 640 + (g & 1) * 320 + (16 + (i & 1)) * 16, 0, 16);
            const int pc = lane >> 4, hc = (nt * 6 + 2 * pc + ((lane >> 3) & 1)) & 31;
            const unsigned val = (lane & 1) ? 0u : 0x3F803F80u;
            if (pc < 3 && ((VMASK >> pc) & 1))
                __builtin_amdgcn_raw_buffer_store_b128(u32x4{val, val, val, val}, rw, (unsigned)(OFF_KV + (hc * 16 + mt) * CHUNK) + 4608 + tg * 1280 + ((lane >> 2) & 1) * 640 + ((lane >> 1) & 1) * 320 + 288 + (lane & 1) * 16, 0, 16);
        }
    }
    QST(6);
    drain_bar();
    if (tid == 0) publish(ws, FL_QKV + nt * 128 + mt * 4, tag);
}
__device__ __forceinline__ void qkv_dispatch(const u16* x, const u16* wq, char* ws, char* lds, int b  , unsigned& tag) {
    const int xc = b & 7, i = b >> 3;
    unsigned touch = 0, tx1 = 0, tx2 = 0, tx3 = 0;
    const int mt = (xc & 3) * 4 + (i & 3), nt = (xc >> 2) * 8 + (i >> 2);
    const int krot = (i * 5) % 18;
    if (nt < 10) qkv_role<0>(x, wq, ws, lds, mt, nt, krot, touch, tx1, tx2, tx3, tag);
    else if (nt == 10) qkv_role<4>(x, wq, ws, lds, mt, nt, krot, touch, tx1, tx2, tx3, tag);
    else qkv_role<7>(x, wq, ws, lds, mt, nt, krot, touch, tx1, tx2, tx3, tag);
}
constexpr int CH_B = 39936, L_K = 6144;
__device__ __forceinline__ void conv_load_u(char* ws, char* lds, int ch, int v, int d) {
    const int lane = threadIdx.x & 63;
    const u32x4 srdu = make_srd(ws + OFF_UT + (size_t)d * 4096, 4096);
    dma16h(srdu, (unsigned)(lane * 64 + v * 16), 0u, rfl((unsigned)(uintptr_t)lds) + ch * CH_B + v * 1536 + 512);
    if (lane < 32) *(__attribute__((address_space(3))) u32x4*)((lds_ptr)lds + ch * CH_B + v * 1536 + lane * 16) = u32x4{0u, 0u, 0u, 0u};
}
__device__ __forceinline__ void conv_load_k(char* ws, char* lds, int ch, int v, int d) {
    const int lane = threadIdx.x & 63;
    const unsigned lds0 = rfl((unsigned)(uintptr_t)lds) + ch * CH_B;
    const u32x4 srdk = make_srd(ws + OFF_KR + (size_t)d * (KRLEN * 2), KRLEN * 2);
    int f = v * 64 + lane, s = 0, c = f;
#pragma unroll
    for (int k = 0; k < 9; k++) {
        const int p = v + 4 * k;
        if (p < 33) dma16h(srdk, (unsigned)((8 * c + (s < 8 ? s : 7)) * 2), 0u, lds0 + L_K + p * 1024);
        c += 256; if (c >= 260) { c -= 260; s++; }
    }
}
__device__ __forceinline__ void conv_compute(char* ws, char* lds, int blk) {
    const int tid = threadIdx.x, lane = tid & 63, w = rfl(tid >> 6);
    const int ch = w >> 2, v = w & 3, d = blk * 2 + ch;
    const lds_ptr lb = (lds_ptr)lds + ch * CH_B;
    const int r = lane & 31, h = lane >> 5, ks = v & 1, par = v >> 1;
    const lds_ptr pa = lb + L_K + ((7 - r) & 7) * 4160 + (((2047 - r) >> 3) + h + 2 * ks - 252) * 16;
    const lds_ptr pb = lb + (2 * ks + h) * 1536 + (r + 1) * 16;
    f32x16 acc0 = {}, acc1 = {};
    auto run = [&](auto PARc) {
        constexpr int PAR = decltype(PARc)::value;
        u32x4 a0, a1, b0, b1, na0, na1, nb0, nb1;
        a0 = lds_r128(pa + 64 * (63 - PAR)); a1 = lds_r128(pa + 64 * (31 - PAR)); b0 = lds_r128(pb + 16 * (31 - PAR)); b1 = lds_r128(pb + 16 * (63 - PAR));
#pragma unroll
        for (int j = 0; j < 16; j++) {
            const int dn = PAR + 2 * (j + 1);
            if (j < 15) { na0 = lds_r128(pa + 64 * (63 - dn)); na1 = lds_r128(pa + 64 * (31 - dn)); nb0 = lds_r128(pb + 16 * (31 - dn)); nb1 = lds_r128(pb + 16 * (63 - dn)); }
            __builtin_amdgcn_sched_barrier(0);
            acc1 = MFMA32(a0, b1, acc1); acc0 = MFMA32(a0, b0, acc0); acc1 = MFMA32(a1, b0, acc1);
            __builtin_amdgcn_sched_barrier(0);
            a0 = na0; a1 = na1; b0 = nb0; b1 = nb1;
        }
    };
    if (par == 0) run(std::integral_constant<int, 0>{}); else run(std::integral_constant<int, 1>{});
    asm volatile("s_waitcnt lgkmcnt(0)\n\ts_barrier" ::: "memory");
    typedef __attribute__((address_space(3))) f32x4* lf4;
    {
        lf4 wp = (lf4)(lb + L_K + v * 8192 + lane * 16);
#pragma unroll
        for (int q = 0; q < 4; q++) { wp[q * 64] = f32x4{acc0[4 * q], acc0[4 * q + 1], acc0[4 * q + 2], acc0[4 * q + 3]}; wp[(4 + q) * 64] = f32x4{acc1[4 * q], acc1[4 * q + 1], acc1[4 * q + 2], acc1[4 * q + 3]}; }
    }
    asm volatile("s_waitcnt lgkmcnt(0)\n\ts_barrier" ::: "memory");
    {
        const int nt = v >> 1, hf = v & 1;
        f32x4 s0 = f32x4{0, 0, 0, 0}, s1 = s0;
#pragma unroll
        for (int o = 0; o < 4; o++) { lf4 rp = (lf4)(lb + L_K + o * 8192 + (nt * 4 + hf * 2) * 1024 + lane * 16); s0 += rp[0]; s1 += rp[64]; }
        const u32x2 y0 = u32x2{cvtpk(s0[0], s0[1]), cvtpk(s0[2], s0[3])}, y1 = u32x2{cvtpk(s1[0], s1[1]), cvtpk(s1[2], s1[3])};
        const auto p0 = __builtin_amdgcn_permlane32_swap(y0[0], y1[0], false, false), p1 = __builtin_amdgcn_permlane32_swap(y0[1], y1[1], false, false);
        __builtin_amdgcn_raw_buffer_store_b128(u32x4{p0[0], p1[0], p0[1], p1[1]}, ws_rsrc(ws, WS_USED), (unsigned)(OFF_YT + d * 4096 + ((nt * 32 + r) * 32 + hf * 16 + 8 * h) * 2), 0, 16);
    }
    asm volatile("s_waitcnt lgkmcnt(0)\n\ts_barrier" ::: "memory");
}
__device__ __forceinline__ void conv_third(char* ws, char* lds, int job, unsigned tag, unsigned pubflag) {
    const int tid = threadIdx.x, lane = tid & 63, w = rfl(tid >> 6);
    const int c = job / 3, th = job - c * 3, d = 512 + c;
    {
        const unsigned lds0 = rfl((unsigned)(uintptr_t)lds);
        const u32x4 srdk = make_srd(ws + OFF_KR + (size_t)d * (KRLEN * 2), KRLEN * 2);
        int f = w * 64 + lane, s = 0, cc = f;
        if (cc >= 260) { cc -= 260; s = 1; }
#pragma unroll
        for (int k = 0; k < 5; k++) {
            const int p = w + 8 * k;
            if (p < 33) dma16h(srdk, (unsigned)((8 * cc + (s < 8 ? s : 7)) * 2), 0u, lds0 + L_K + p * 1024);
            cc += 512; if (cc >= 260) { cc -= 260; s++; } if (cc >= 260) { cc -= 260; s++; }
        }
        if (w < 4) conv_load_u(ws, lds, 0, w, d);
    }
    const int r = lane & 31, h = lane >> 5;
    const lds_ptr pa = (lds_ptr)lds + L_K + ((7 - r) & 7) * 4160 + (((2047 - r) >> 3) + h - 252) * 16;
    const lds_ptr pb = (lds_ptr)lds + h * 1536 + (r + 1) * 16;
    asm volatile("s_waitcnt vmcnt(0) lgkmcnt(0)\n\ts_barrier" ::: "memory");
    if (tid == 0) publish(ws, pubflag, tag);
    f32x16 acc0 = {}, acc1 = {};
    const int first = th == 0 ? 0 : 4 + 20 * th, last = 24 + 20 * th;
    typedef __attribute__((address_space(3))) f32x4* lf4;
    if (w < 4) {
        for (int n = first + w; n < last; n += 4) {
            const int ks = n & 1, dl = n >> 1;
            const lds_ptr qa = pa + ks * 32 - 64 * dl, qb = pb + ks * 3072 - 16 * dl;
            const u32x4 a0 = lds_r128(qa + 64 * 63), a1 = lds_r128(qa + 64 * 31), b0 = lds_r128(qb + 16 * 31), b1 = lds_r128(qb + 16 * 63);
            acc1 = MFMA32(a0, b1, acc1); acc0 = MFMA32(a0, b0, acc0); acc1 = MFMA32(a1, b0, acc1);
        }
        lf4 wp = (lf4)((lds_ptr)lds + CH_B + w * 8192 + lane * 16);
#pragma unroll
        for (int q = 0; q < 4; q++) { wp[q * 64] = f32x4{acc0[4 * q], acc0[4 * q + 1], acc0[4 * q + 2], acc0[4 * q + 3]}; wp[(4 + q) * 64] = f32x4{acc1[4 * q], acc1[4 * q + 1], acc1[4 * q + 2], acc1[4 * q + 3]}; }
    }
    asm volatile("s_waitcnt lgkmcnt(0)\n\ts_barrier" ::: "memory");
    {
        f32x4 sum = f32x4{0, 0, 0, 0};
#pragma unroll
        for (int o = 0; o < 4; o++) sum += *(lf4)((lds_ptr)lds + CH_B + o * 8192 + w * 1024 + lane * 16);
        const int l0 = ((w >> 2) * 32 + r) * 32 + (w & 3) * 8 + 4 * h;
        __builtin_amdgcn_raw_buffer_store_b128(__builtin_bit_cast(u32x4, sum), ws_rsrc(ws, WS_USED), (unsigned)(OFF_YP + (job * 2048 + l0) * 4), 0, 16);
    }
    drain_bar();
    if (tid == 0) publish(ws, FL_LEFT + job * 4, tag);
}
#define PRIO_AUX2 3
#define AUX_KS 4
#define UT_SPINS (1u << 20)
#define UT_SLEEP 12
#define LDSF() asm volatile("s_waitcnt lgkmcnt(0)" ::: "memory")
#define STX(i)
#define AUX_PRIO 0
constexpr int AUX_BLOCKS = 256;
struct cplx { float re, im; };
__device__ __forceinline__ cplx cmul(cplx a, cplx b) { return cplx{a.re * b.re - a.im * b.im, a.re * b.im + a.im * b.re}; }
__device__ __forceinline__ cplx cexp_dir(float a2, float br, float x) {
    const float e = __builtin_amdgcn_exp2f(a2 * x), t = __builtin_amdgcn_fractf(br * x);
    return cplx{e * __builtin_amdgcn_cosf(t), e * __builtin_amdgcn_sinf(t)};
}
__device__ __forceinline__ void aux_role(const u16* u, const u16* Are, const u16* Aim, const u16* Bre, const u16* Bim, const u16* Cre, const u16* Cim, const u16* logdt,
                                         char* ws, char* lds, int blk, unsigned& tag) {
    const int tid = threadIdx.x, lane = tid & 63, w = rfl(tid >> 6);
    const int i = lane & 15, g = lane >> 4;
    typedef __attribute__((address_space(3))) unsigned* lu32; typedef __attribute__((address_space(3))) u16* lu16; typedef __attribute__((address_space(3))) f32x4* lf4;
    const int ch = w >> 1, par = w & 1, d = ch < 2 ? blk * 2 + ch : 512 + blk;
    constexpr int L_TAB = 54272, L_PART = L_TAB + 6 * 1024;
    const int l_kt = ch == 0 ? 20480 : ch == 1 ? 73984 : 69632;
    STX(0);
    f32x4 acc[3];
    const bool kgen = w < 4 || (w < 6 && blk < 64);
    if (w >= 6) {
        __builtin_amdgcn_s_setprio(3);
        const int x = w - 6, lt = blk >> 3, cg8 = blk & 7, cpr = x ? 3 : 6;
        const u32x4 srd = make_srd(u, 2048 * 576 * 2);
        const unsigned lds0 = rfl((unsigned)(uintptr_t)lds) + x * 8192;
#pragma unroll
        for (int p = 0; p < 6; p++)
            if (p < cpr) {
                const int f = p * 64 + lane, row = x ? (f * 21846) >> 16 : (f * 10923) >> 16, c = f - row * cpr;
                dma16(srd, (unsigned)((lt * 64 + row) * 1152 + cg8 * 144 + x * 96 + c * 16), 0u, lds0 + p * 1024);
            }
        tag = read_tag(ws);
        asm volatile("s_waitcnt vmcnt(0)" ::: "memory");
        const int q = i >> 2, p = i & 3;
#pragma unroll
        for (int z = 0; z < 6; z++)
            if (z < cpr - (x ? -1 : 0)) {
                const int cb = x ? (z >> 1) * 8 : (z >> 1) * 16, l0 = (g + 4 * (z & 1)) * 8;
                const lds_ptr a = (lds_ptr)lds + x * 8192 + (l0 + q) * (cpr * 16) + (cb + 4 * p) * 2;
                const u32x2 lo = lds_tr(a), hi = lds_tr(a + 4 * cpr * 16);
                __builtin_amdgcn_raw_buffer_store_b128(u32x4{lo[0], lo[1], hi[0], hi[1]}, ws_rsrc(ws, WS_USED), (unsigned)(OFF_UT + (cg8 * 72 + x * 48 + cb + i) * 4096 + (lt * 64 + l0) * 2), 0, 16);
            }
        asm volatile("s_waitcnt vmcnt(0)" ::: "memory");
        if (lane == 0) publish(ws, FL_UT + cg8 * 256 + (lt * 2 + x) * 4, tag);
    } else if (kgen) {
        const int n = par * 64 + lane, o = d * 128 + n;
        const float dt = bf2f(f2bf(__expf(bf2f(logdt[d]))));
        const float a = dt * bf2f(Are[o]), b = dt * bf2f(Aim[o]);
        const float br_ = dt * bf2f(Bre[o]), bi_ = dt * bf2f(Bim[o]), cr = bf2f(Cre[o]), ci = bf2f(Cim[o]);
        const cplx W = cplx{cr * br_ - ci * bi_, cr * bi_ + ci * br_};
        const float a2 = a * 1.4426950408889634f, br = b * 0.15915494309189535f;
        STX(1);
        if (w == 0) tag = read_tag(ws);
        typedef __attribute__((address_space(3))) f32x4* lp4;
        const lds_ptr tp = (lds_ptr)lds + L_TAB + w * 1024;
        *(lp4)(tp + lane * 16) = f32x4{a2, br, 0.5f * __builtin_amdgcn_logf(W.re * W.re + W.im * W.im), atan2f(W.im, W.re) * 0.15915494309189535f};
        LDSF();
        const float xy = 8.f * i, x0 = (float)(128 * (i >> 3) + (i & 7));
        const float x1 = i < 8 ? (float)(256 + 128 * (i >> 2) + 2 * (i & 3)) : (float)(512 + 128 * ((i - 8) >> 1) + 4 * (i & 1)), x2 = (float)(1024 + 128 * (i < 9 ? i : 8));
        f32x4 s0 = f32x4{0, 0, 0, 0}, s1 = s0, s2 = s0;
#pragma unroll
        for (int ks = 0; ks < AUX_KS; ks++) {
            u32x4 fy, f0, f1, f2;
#pragma unroll
            for (int m = 0; m < 4; m++) {
                const f32x4 pm = *(lp4)(tp + (ks * 16 + g * 4 + m) * 16);
                auto val = [&](float x, float lw, float ph, float sg) {
                    const float e = __builtin_amdgcn_exp2f(__builtin_fmaf(pm[0], x, lw)), t = __builtin_amdgcn_fractf(__builtin_fmaf(pm[1], x, ph));
                    return cvtpk(e * __builtin_amdgcn_cosf(t), sg * e * __builtin_amdgcn_sinf(t)); };
                fy[m] = val(xy, 0.f, 0.f, -1.f);
                f0[m] = val(x0, pm[2], pm[3], 1.f); f1[m] = val(x1, pm[2], pm[3], 1.f); f2[m] = val(x2, pm[2], pm[3], 1.f);
            }
            s0 = MFMA16(f0, fy, s0); s1 = MFMA16(f1, fy, s1); s2 = MFMA16(f2, fy, s2);
        }
        acc[0] = s0; acc[1] = s1; acc[2] = s2;
        STX(2);
        lf4 pp = (lf4)((lds_ptr)lds + L_PART + ch * 3072 + lane * 16);
        if (par) { pp[0] = acc[0]; pp[64] = acc[1]; } else pp[128] = acc[2];
    }
    asm volatile("s_waitcnt vmcnt(0) lgkmcnt(0)\n\ts_barrier" ::: "memory");
    STX(3);
    __builtin_amdgcn_s_setprio(PRIO_AUX2);
    if (w >= 6) {
        for (unsigned spins = 0; spins < (1u << 20); ++spins) {
            if (__all(__hip_atomic_load(flag_ptr(ws, FL_UT + ((blk * 2) / 72) * 256 + lane * 4), RLX_AGENT) == tag)) break;
            __builtin_amdgcn_s_sleep(UT_SLEEP);
        }
#pragma unroll
        for (int v = 0; v < 4; v++) conv_load_u(ws, lds, w - 6, v, blk * 2 + (w - 6));
    }
    if (kgen) {
        lf4 pp = (lf4)((lds_ptr)lds + L_PART + ch * 3072 + lane * 16);
        lu16 kt = (lu16)((lds_ptr)lds + l_kt);
        if (par == 0) {
            const f32x4 s0 = acc[0] + pp[0], s1 = acc[1] + pp[64];
            const int x0 = 128 * (g >> 1) + 4 * (g & 1) + 8 * i;
            const int x1 = (g < 2 ? 256 + 128 * g : 512 + 256 * (g - 2)) + 8 * i;
#pragma unroll
            for (int r = 0; r < 4; r++) {
                kt[x0 + r] = f2bf(s0[r]);
                kt[x1 + (g < 2 ? 2 * r : 128 * (r >> 1) + 4 * (r & 1))] = f2bf(s1[r]);
            }
        } else {
            const f32x4 s2 = acc[2] + pp[128];
#pragma unroll
            for (int r = 0; r < 4; r++) { const int r0 = 4 * g + r; if (r0 < 9) kt[1024 + 128 * r0 + 8 * i] = f2bf(s2[r]); }
        }
    }
    asm volatile("s_waitcnt lgkmcnt(0)\n\ts_barrier" ::: "memory");
    STX(4);
    if (kgen) {
        const lu16 kt = (lu16)((lds_ptr)lds + l_kt);
#pragma unroll
        for (int k = 0; k < 3; k++) {
            const int c = k * 128 + par * 64 + lane;
            if (c < 261) {
                u32x4 o = u32x4{0u, 0u, 0u, 0u};
                if (c < 256) {
                    const int m = 255 - c;
                    const unsigned pat = m < 32 ? 0x01234567u : m < 64 ? 0x00244468u : m < 128 ? 0x00044488u : ((m & 1) ? 0x00008888u : 0x00000888u);
                    unsigned v[8];
#pragma unroll
                    for (int e = 0; e < 8; e++) v[e] = kt[8 * m + ((pat >> (4 * e)) & 15)];
                    o = u32x4{v[0] | (v[1] << 16), v[2] | (v[3] << 16), v[4] | (v[5] << 16), v[6] | (v[7] << 16)};
                }
                if (ch == 2) __builtin_amdgcn_raw_buffer_store_b128(o, ws_rsrc(ws, WS_USED), (unsigned)(OFF_KR + d * (KRLEN * 2) + c * 16), 0, 16);
                else if (c < 260) *(__attribute__((address_space(3))) u32x4*)((lds_ptr)lds + ch * CH_B + L_K + c * 16) = o;
            }
        }
    }
    asm volatile("s_waitcnt lgkmcnt(0)\n\ts_barrier" ::: "memory");
    {
        const int c2 = w >> 2, v = w & 3;
        const lds_ptr kb = (lds_ptr)lds + c2 * CH_B + L_K;
#pragma unroll
        for (int k = 0; k < 2; k++) {
            const int c = k * 256 + v * 64 + lane;
            if (c < 260) {
                u32x4 lo = u32x4{0u, 0u, 0u, 0u}, hi = lo;
                if (c < 256) { lo = lds_r128(kb + c * 16); hi = lds_r128(kb + c * 16 + 16); }
                const unsigned wv[8] = {lo[0], lo[1], lo[2], lo[3], hi[0], hi[1], hi[2], hi[3]};
                unsigned xv[7];
#pragma unroll
                for (int j = 0; j < 7; j++) xv[j] = __builtin_amdgcn_alignbit(wv[j + 1], wv[j], 16);
#pragma unroll
                for (int s = 1; s < 8; s++) {
                    const int o = s >> 1;
                    *(__attribute__((address_space(3))) u32x4*)(kb + s * 4160 + c * 16) = (s & 1) ? u32x4{xv[o], xv[o + 1], xv[o + 2], xv[o + 3]} : u32x4{wv[o], wv[o + 1], wv[o + 2], wv[o + 3]};
                }
            }
        }
    }
    STX(5);
    drain_bar();
    if (tid == 0) publish(ws, FL_KG + blk * 4, tag);
}
#define OPST(i)
constexpr int YT_LDS = 73728;
#define STO(i)
__device__ __forceinline__ int wo_row(int kp) {
    if (kp < 512) { const int h = kp >> 4, hh = (kp >> 3) & 1, jj = kp & 7; return 18 * h + (jj & 3) + 8 * (jj >> 2) + 4 * hh + 2 * (h & 1); }
    const int h = (kp - 512) >> 1, e = kp & 1; return 18 * h + ((h & 1) ? e : 16 + e);
}
__device__ __forceinline__ void oproj_prefetch(const u16* wo, char* lds, int nt) {
    const int lane = threadIdx.x & 63, w = rfl(threadIdx.x >> 6);
    const u32x4 srdw = make_srd(wo, 576 * 576 * 2);
    const unsigned lds0 = rfl((unsigned)(uintptr_t)lds);
    const int k2 = 8 * w + (lane >> 3), pos = lane & 7, k5 = k2 & 31, sg = ((k5 >> 1) & 1) | (((k5 >> 3) & 1) << 1);
    const unsigned col = (unsigned)(nt * 128 + (((pos >> 1) ^ sg) * 2 + (pos & 1)) * 16);
    const unsigned vm = (unsigned)(wo_row(k2) * 1152) + col, vx = (unsigned)(wo_row(512 + k2) * 1152) + col;
#pragma unroll
    for (int k = 0; k < 8; k++) dma16(srdw, vm, (unsigned)(k * 72 * 1152), lds0 + (w + 8 * k) * 1024);
    dma16(srdw, vx, 0u, lds0 + (w + 64) * 1024);
}
__device__ __forceinline__ void oproj_tile(char* ws, u16* out, char* lds, int mt, int nt, unsigned tag) {
    const int lane = threadIdx.x & 63, w = rfl(threadIdx.x >> 6), tg = w & 3, hi = w >> 2;
    const int c0 = hi * 2;
    constexpr int NC = 2;
    const unsigned xo = (unsigned)(OFF_O + (mt * 4 + tg) * 18432);
    const int i = lane & 15, g = lane >> 4, q = i >> 2, p = i & 3, k = 8 * g + q, sg = ((k >> 1) & 1) | (((k >> 3) & 1) << 1);
    lds_ptr wa[NC]; f32x4 acc[NC]; u32x4 wf[3][NC];
#pragma unroll
    for (int c = 0; c < NC; c++) { wa[c] = (lds_ptr)lds + k * 128 + (((c0 + c) ^ sg) * 32) + p * 8; acc[c] = f32x4{0, 0, 0, 0}; }
    auto wfrag = [&](int c, int ks) { const u32x2 l0 = lds_tr(wa[c] + ks * 4096), h0 = lds_tr(wa[c] + ks * 4096 + 512); return u32x4{l0[0], l0[1], h0[0], h0[1]}; };
#pragma unroll
    for (int s = 0; s < 3; s++)
#pragma unroll
        for (int c = 0; c < NC; c++) wf[s][c] = wfrag(c, s);
    asm volatile("s_waitcnt lgkmcnt(0)" : "+v"(wf[0][0]), "+v"(wf[0][1]), "+v"(wf[1][0]), "+v"(wf[1][1]), "+v"(wf[2][0]), "+v"(wf[2][1]));
    const unsigned mw = rfl((unsigned)(uintptr_t)lds) + YT_LDS;
    const char* fa = ws + FL_ATT + (mt >> 2) * 128 + (lane & 31) * 4;
    const u32x4 srd = make_srd(ws, WS_USED);
    u32x4 xf[18];
    {
        unsigned todo, tx, cnt, hm, a_, n_, t_;
        asm volatile(
        "s_mov_b32 %[todo], 0x55555555\n"
        "s_mov_b32 %[tx], 3\n"
        "s_mov_b32 %[cnt], 0\n"
        "1:\n"
        "s_cmp_eq_u32 %[w], 0\n"
        "s_cbranch_scc0 2f\n"
        "global_load_dword v112, %[fa], off sc1\n"
        "s_waitcnt vmcnt(0)\n"
        "v_cmp_eq_u32 vcc, %[tag], v112\n"
        "s_nop 1\n"
        "v_mov_b32 v112, vcc_lo\n"
        "v_mov_b32 v113, vcc_hi\n"
        "s_mov_b32 %[hm], vcc_lo\n"
        "s_and_b32 %[a], vcc_lo, vcc_hi\n"
        "ds_write_b64 %[mw], v[112:113]\n"
        "s_branch 3f\n"
        "2:\n"
        "ds_read_b32 v112, %[mr]\n"
        "s_mov_b32 %[a], -1\n"
        "s_waitcnt lgkmcnt(0)\n"
        "s_nop 0\n"
        "v_readfirstlane_b32 %[hm], v112\n"
        "3:\n"
        "s_nop 3\n"
        "s_lshr_b32 %[n], %[hm], 1\n"
        "s_and_b32 %[n], %[n], %[hm]\n"
        "s_and_b32 %[n], %[n], %[todo]\n"
        "s_andn2_b32 %[todo], %[todo], %[n]\n"
        "s_bitcmp1_b32 %[n], 0\n"
        "s_cbranch_scc0 100f\n"
        "s_add_u32 %[t], %[xo], 0\n"
        "buffer_load_dwordx4 v[40:43], %[vo], %[srd], %[t] offen sc1\n"
        "100:\n"
        "s_bitcmp1_b32 %[n], 2\n"
        "s_cbranch_scc0 101f\n"
        "s_add_u32 %[t], %[xo], 1024\n"
        "buffer_load_dwordx4 v[44:47], %[vo], %[srd], %[t] offen sc1\n"
        "101:\n"
        "s_bitcmp1_b32 %[n], 4\n"
        "s_cbranch_scc0 102f\n"
        "s_add_u32 %[t], %[xo], 2048\n"
        "buffer_load_dwordx4 v[48:51], %[vo], %[srd], %[t] offen sc1\n"
        "102:\n"
        "s_bitcmp1_b32 %[n], 6\n"
        "s_cbranch_scc0 103f\n"
        "s_add_u32 %[t], %[xo], 3072\n"
        "buffer_load_dwordx4 v[52:55], %[vo], %[srd], %[t] offen sc1\n"
        "103:\n"
        "s_bitcmp1_b32 %[n], 8\n"
        "s_cbranch_scc0 104f\n"
        "s_add_u32 %[t], %[xo], 4096\n"
        "buffer_load_dwordx4 v[56:59], %[vo], %[srd], %[t] offen sc1\n"
        "104:\n"
        "s_bitcmp1_b32 %[n], 10\n"
        "s_cbranch_scc0 105f\n"
        "s_add_u32 %[t], %[xo], 5120\n"
        "buffer_load_dwordx4 v[60:63], %[vo], %[srd], %[t] offen sc1\n"
        "105:\n"
        "s_bitcmp1_b32 %[n], 12\n"
        "s_cbranch_scc0 106f\n"
        "s_add_u32 %[t], %[xo], 6144\n"
        "buffer_load_dwordx4 v[64:67], %[vo], %[srd], %[t] offen sc1\n"
        "106:\n"
        "s_bitcmp1_b32 %[n], 14\n"
        "s_cbranch_scc0 107f\n"
        "s_add_u32 %[t], %[xo], 7168\n"
        "buffer_load_dwordx4 v[68:71], %[vo], %[srd], %[t] offen sc1\n"
        "107:\n"
        "s_bitcmp1_b32 %[n], 16\n"
        "s_cbranch_scc0 108f\n"
        "s_add_u32 %[t], %[xo], 8192\n"
        "buffer_load_dwordx4 v[72:75], %[vo], %[srd], %[t] offen sc1\n"
        "108:\n"
        "s_bitcmp1_b32 %[n], 18\n"
        "s_cbranch_scc0 109f\n"
        "s_add_u32 %[t], %[xo], 9216\n"
        "buffer_load_dwordx4 v[76:79], %[vo], %[srd], %[t] offen sc1\n"
        "109:\n"
        "s_bitcmp1_b32 %[n], 20\n"
        "s_cbranch_scc0 110f\n"
        "s_add_u32 %[t], %[xo], 10240\n"
        "buffer_load_dwordx4 v[80:83], %[vo], %[srd], %[t] offen sc1\n"
        "110:\n"
        "s_bitcmp1_b32 %[n], 22\n"
        "s_cbranch_scc0 111f\n"
        "s_add_u32 %[t], %[xo], 11264\n"
        "buffer_load_dwordx4 v[84:87], %[vo], %[srd], %[t] offen sc1\n"
        "111:\n"
        "s_bitcmp1_b32 %[n], 24\n"
        "s_cbranch_scc0 112f\n"
        "s_add_u32 %[t], %[xo], 12288\n"
        "buffer_load_dwordx4 v[88:91], %[vo], %[srd], %[t] offen sc1\n"
        "112:\n"
        "s_bitcmp1_b32 %[n], 26\n"
        "s_cbranch_scc0 113f\n"
        "s_add_u32 %[t], %[xo], 13312\n"
        "buffer_load_dwordx4 v[92:95], %[vo], %[srd], %[t] offen sc1\n"
        "113:\n"
        "s_bitcmp1_b32 %[n], 28\n"
        "s_cbranch_scc0 114f\n"
        "s_add_u32 %[t], %[xo], 14336\n"
        "buffer_load_dwordx4 v[96:99], %[vo], %[srd], %[t] offen sc1\n"
        "114:\n"
        "s_bitcmp1_b32 %[n], 30\n"
        "s_cbranch_scc0 115f\n"
        "s_add_u32 %[t], %[xo], 15360\n"
        "buffer_load_dwordx4 v[100:103], %[vo], %[srd], %[t] offen sc1\n"
        "115:\n"
        "s_and_b32 %[t], %[hm], 0xffff\n"
        "s_cmp_eq_u32 %[t], 0xffff\n"
        "s_cselect_b32 %[n], 1, 0\n"
        "s_lshr_b32 %[t], %[hm], 16\n"
        "s_cmp_eq_u32 %[t], 0xffff\n"
        "s_cselect_b32 %[t], 2, 0\n"
        "s_or_b32 %[n], %[n], %[t]\n"
        "s_and_b32 %[n], %[n], %[tx]\n"
        "s_andn2_b32 %[tx], %[tx], %[n]\n"
        "s_bitcmp1_b32 %[n], 0\n"
        "s_cbranch_scc0 116f\n"
        "s_add_u32 %[t], %[xo], 16384\n"
        "buffer_load_dwordx4 v[104:107], %[vo], %[srd], %[t] offen sc1\n"
        "116:\n"
        "s_bitcmp1_b32 %[n], 1\n"
        "s_cbranch_scc0 117f\n"
        "s_add_u32 %[t], %[xo], 17408\n"
        "buffer_load_dwordx4 v[108:111], %[vo], %[srd], %[t] offen sc1\n"
        "117:\n"
        "s_or_b32 %[t], %[todo], %[tx]\n"
        "s_not_b32 %[a], %[a]\n"
        "s_or_b32 %[t], %[t], %[a]\n"
        "s_cmp_eq_u32 %[t], 0\n"
        "s_cbranch_scc1 4f\n"
        "s_add_u32 %[cnt], %[cnt], 1\n"
        "s_cmp_lt_u32 %[cnt], 0x400000\n"
        "s_cbranch_scc0 4f\n"
        "s_cmp_eq_u32 %[w], 0\n"
        "s_cbranch_scc1 5f\n"
        "s_sleep 2\n"
        "s_branch 1b\n"
        "5:\n"
        "s_bcnt1_i32_b32 %[t], %[hm]\n"
        "s_cmp_lt_u32 %[t], 4\n"
        "s_cbranch_scc1 6f\n"
        "s_sleep 2\n"
        "s_branch 1b\n"
        "6:\n"
        "s_sleep 24\n"
        "s_branch 1b\n"
        "4:\n"
        "s_waitcnt vmcnt(0) lgkmcnt(0)\n"
        : "={v[40:43]}"(xf[0]), "={v[44:47]}"(xf[1]), "={v[48:51]}"(xf[2]), "={v[52:55]}"(xf[3]), "={v[56:59]}"(xf[4]), "={v[60:63]}"(xf[5]), "={v[64:67]}"(xf[6]), "={v[68:71]}"(xf[7]), "={v[72:75]}"(xf[8]), "={v[76:79]}"(xf[9]), "={v[80:83]}"(xf[10]), "={v[84:87]}"(xf[11]), "={v[88:91]}"(xf[12]), "={v[92:95]}"(xf[13]), "={v[96:99]}"(xf[14]), "={v[100:103]}"(xf[15]), "={v[104:107]}"(xf[16]), "={v[108:111]}"(xf[17]),
          [todo] "=&s"(todo), [tx] "=&s"(tx), [cnt] "=&s"(cnt), [hm] "=&s"(hm), [a] "=&s"(a_), [n] "=&s"(n_), [t] "=&s"(t_)
        : [w] "s"(w), [fa] "v"(fa), [tag] "s"(tag), [mw] "v"(mw), [mr] "v"(mw), [xo] "s"(xo), [vo] "v"(lane * 16), [srd] "s"(srd)
        : "memory", "scc", "vcc", "v112", "v113");
    }
    OPST(2);
#pragma unroll
    for (int ks = 0; ks < 18; ks++) {
#pragma unroll
        for (int c = 0; c < NC; c++) acc[c] = MFMA16(wf[ks % 3][c], xf[ks], acc[c]);
        if (ks + 3 < 18) {
#pragma unroll
            for (int c = 0; c < NC; c++) wf[ks % 3][c] = wfrag(c, ks + 3);
        }
        __builtin_amdgcn_sched_barrier(0);
    }
    OPST(3);
    int lane2 = threadIdx.x & 63; asm volatile("" : "+v"(lane2));
    const int i2 = lane2 & 15, g2 = lane2 >> 4;
#pragma unroll
    for (int c = 0; c < NC; c += 2) {
        const f32x4 a = acc[c], b = acc[c + 1];
        const unsigned a0 = cvtpk(a[0], a[1]), a1 = cvtpk(a[2], a[3]), b0 = cvtpk(b[0], b[1]), b1 = cvtpk(b[2], b[3]);
        const auto p0 = __builtin_amdgcn_permlane16_swap(a0, b0, false, false), p1 = __builtin_amdgcn_permlane16_swap(a1, b1, false, false);
        __builtin_amdgcn_raw_buffer_store_b128(u32x4{p0[0], p1[0], p0[1], p1[1]}, ws_rsrc(out, 2048 * 1152 * 2), (unsigned)(((mt * 64 + tg * 16 + i2) * 1152 + 576 + nt * 64 + (c0 + c) * 16 + (g2 & 1) * 16 + (g2 >> 1) * 8) * 2), 0, 16);
    }
}
__device__ __forceinline__ void ytrans_role(const u16* u, const u16* dskip, const char* ws, u16* out, char* lds, int t) {
    const int tid = threadIdx.x, lane = tid & 63, w = rfl(tid >> 6), i = lane & 15, g = lane >> 4;
    const int hf = w & 1, lt = t / 9, ct = t - lt * 9;
    if (ct == 8) {
        const int chn = tid >> 3, oc = tid & 7;
        const __amdgpu_buffer_rsrc_t rw = ws_rsrc(ws, WS_USED);
        f32x4 s0 = f32x4{0, 0, 0, 0}, s1 = s0;
#pragma unroll
        for (int q = 0; q < 3; q++) {
            const unsigned o = (unsigned)(OFF_YP + ((chn * 3 + q) * 2048 + lt * 64 + oc * 8) * 4);
            s0 += __builtin_bit_cast(f32x4, __builtin_amdgcn_raw_buffer_load_b128(rw, o, 0, 16)); s1 += __builtin_bit_cast(f32x4, __builtin_amdgcn_raw_buffer_load_b128(rw, o + 16, 0, 16));
        }
        *(__attribute__((address_space(3))) u32x4*)((lds_ptr)lds + YT_LDS + (oc >> 2) * 4096 + chn * 64 + (oc & 3) * 16) = u32x4{cvtpk(s0[0], s0[1]), cvtpk(s0[2], s0[3]), cvtpk(s1[0], s1[1]), cvtpk(s1[2], s1[3])};
        asm volatile("s_waitcnt lgkmcnt(0)\n\ts_barrier" ::: "memory");
    }
    if (w >= 2) return;
    if (ct < 8) {
        const u32x4 srd = make_srd(ws + OFF_YT, 576 * 4096);
        const unsigned lds0 = rfl((unsigned)(uintptr_t)lds);
#pragma unroll
        for (int k = 0; k < 4; k++)
            dma16h(srd, (unsigned)((ct * 64 + k * 16 + (lane >> 2)) * 4096 + (lt * 64 + hf * 32) * 2 + (lane & 3) * 16), 0u, lds0 + YT_LDS + w * 4096 + k * 1024);
    }
    u32x4 uu[4]; u32x4 dk[2];
#pragma unroll
    for (int o = 0; o < 2; o++) dk[o] = *(const u32x4*)(dskip + ct * 64 + (g + 4 * o) * 8);
#pragma unroll
    for (int k = 0; k < 4; k++) { const int lb = k >> 1, o = k & 1; uu[k] = *(const u32x4*)(u + (size_t)(lt * 64 + hf * 32 + lb * 16 + i) * 576 + ct * 64 + (g + 4 * o) * 8); }
    asm volatile("s_waitcnt vmcnt(0)" ::: "memory");
    const int q = i >> 2, p = i & 3;
#pragma unroll
    for (int k = 0; k < 4; k++) {
        const int lb = k >> 1, o = k & 1, c0 = (g + 4 * o) * 8;
        const lds_ptr a = (lds_ptr)lds + YT_LDS + w * 4096 + (c0 + q) * 64 + (lb * 16 + 4 * p) * 2;
        const u32x2 lo = lds_tr(a), hi = lds_tr(a + 256);
        const unsigned y[4] = {lo[0], lo[1], hi[0], hi[1]};
        u32x4 r;
#pragma unroll
        for (int j = 0; j < 4; j++) {
            const float u0 = __uint_as_float(uu[k][j] << 16), u1 = __uint_as_float(uu[k][j] & 0xffff0000u);
            const float d0 = __uint_as_float(dk[o][j] << 16), d1 = __uint_as_float(dk[o][j] & 0xffff0000u);
            const unsigned s = cvtpk(u0 * d0, u1 * d1);
            r[j] = cvtpk(__uint_as_float(y[j] << 16) + __uint_as_float(s << 16), __uint_as_float(y[j] & 0xffff0000u) + __uint_as_float(s & 0xffff0000u));
        }
        __builtin_amdgcn_raw_buffer_store_b128(r, ws_rsrc(out, 2048 * 1152 * 2), (unsigned)(((lt * 64 + hf * 32 + lb * 16 + i) * 1152 + ct * 64 + c0) * 2), 0, 16);
    }
}
#define PRIO_OP 2
#define PRIO_CONV_LATE 2
#define ATT_SLEEP 4
#define YT_SLEEP 8
#define PRIO_AUX 0
#define PRIO_YT 2
#define PRIO_BG 2
#define PRIO_QKV 3
#define PRIO_CONV 2
#define PRIO_ATT 3
constexpr int LDSB = 81920;
NOPK __global__ __launch_bounds__(512, 4) void fused(const u16* u, const u16* Are, const u16* Aim, const u16* Bre, const u16* Bim, const u16* Cre, const u16* Cim, const u16* logdt,
                                                     const u16* dskip, const u16* x, const u16* wq, const u16* wo, u16* out, char* ws) {
    extern __shared__ __attribute__((aligned(16))) char lds[];
    FST(0);
    unsigned tagv = 0;
    const int b = blockIdx.x, lane = threadIdx.x & 63;
    if (b < 256) {
        __builtin_amdgcn_s_setprio(PRIO_QKV);
        qkv_dispatch(x, wq, ws, lds, b, tagv);
        const unsigned tag = rfl(tagv);
        FST(1);
        __builtin_amdgcn_s_setprio(PRIO_ATT);
        const int xc = b & 7, i = b >> 3, h = xc * 4 + (i >> 3), qb = i & 7;
        { const int ntq = h / 6, ntk = (32 + h) / 6, ntv = (64 + h) / 6;
          wait_flags<1>(ws, lane < 16 ? FL_QKV + ntk * 128 + lane * 4 : lane < 32 ? FL_QKV + ntv * 128 + (lane - 16) * 4 : lane < 34 ? FL_QKV + ntq * 128 + (2 * qb + lane - 32) * 4 : 0u, tag); }
        FST(4);
        attn_role(ws, lds, h, qb, tag);
        FST(5);
        if (xc == 0) {
            const int nt = i < 20 ? 4 + (i >> 2) : (i - 20) / 3, mt = i < 20 ? 28 + (i & 3) : 29 + (i - 20) % 3;
            oproj_prefetch(wo, lds, nt);
            if (threadIdx.x < 2) *(__attribute__((address_space(3))) volatile unsigned*)((lds_ptr)lds + YT_LDS + threadIdx.x * 4) = 0u;
            asm volatile("s_waitcnt vmcnt(0) lgkmcnt(0)\n\ts_barrier" ::: "memory");
            oproj_tile(ws, out, lds, mt, nt, tag);
        }
        FST(6);
    } else {
        const int hb = b - 256;
        __builtin_amdgcn_s_setprio(PRIO_AUX);
        aux_role(u, Are, Aim, Bre, Bim, Cre, Cim, logdt, ws, lds, hb, tagv);
        const unsigned tag = rfl(tagv);
        HFST(1);
        if (hb < 64) __builtin_amdgcn_s_setprio(PRIO_CONV_LATE); else __builtin_amdgcn_s_setprio(PRIO_CONV);
        HFST(6);
        conv_compute(ws, lds, hb);
        __builtin_amdgcn_s_setprio(PRIO_BG);
        HFST(7);
        if (hb >= 64) {
            wait_flags<8>(ws, FL_UT + 7 * 256 + lane * 4, tag);
            wait_flags<8>(ws, lane == 0 ? FL_KG + ((hb - 64) / 3) * 4 : 0u, tag);
            conv_third(ws, lds, hb - 64, tag, FL_CONV + (hb >> 5) * 128 + (hb & 31) * 4);
        } else {
            drain_bar();
            if (threadIdx.x == 0) publish(ws, FL_CONV + (hb >> 5) * 128 + (hb & 31) * 4, tag);
        }
        HFST(2);
        __builtin_amdgcn_s_setprio(PRIO_YT);
        const int nt = hb % 9, mt = hb / 9;
        oproj_prefetch(wo, lds, nt);
        auto ytile = [&](int t) __attribute__((always_inline)) {
            if (t % 9 < 8) wait_flags<YT_SLEEP>(ws, lane < 32 ? FL_CONV + (t % 9) * 128 + lane * 4 : 0u, tag);
            else for (int z = 0; z < 3; z++) wait_flags<YT_SLEEP>(ws, FL_LEFT + (z * 64 + lane) * 4, tag);
            ytrans_role(u, dskip, ws, out, lds, t);
            asm volatile("s_waitcnt vmcnt(0) lgkmcnt(0)\n\ts_barrier" ::: "memory"); };
        ytile(hb);
        if (hb >= 128 && hb < 160) ytile(128 + hb);
        HFST(3);
        if (hb == 255 && threadIdx.x < 64) {
            for (unsigned spins = 0; spins < (1u << 20); ++spins) {
                bool ok = true;
#pragma unroll
                for (int k = 0; k < 4; k++) { const int j = k * 64 + lane; ok = ok && __hip_atomic_load(flag_ptr(ws, FL_QKV + (j >> 4) * 128 + (j & 15) * 4), RLX_AGENT) == tag;
                    ok = ok && __hip_atomic_load(flag_ptr(ws, FL_KG + j * 4), RLX_AGENT) == tag; }
                if (__all(ok)) break;
                __builtin_amdgcn_s_sleep(8);
            }
            if (lane == 0) publish(ws, FL_GEN, tag);
        }
        if (threadIdx.x < 2) *(__attribute__((address_space(3))) volatile unsigned*)((lds_ptr)lds + YT_LDS + threadIdx.x * 4) = 0u;
        asm volatile("s_waitcnt lgkmcnt(0)\n\ts_barrier" ::: "memory");
        __builtin_amdgcn_s_setprio(PRIO_OP);
        HFST(4);
        oproj_tile(ws, out, lds, mt, nt, tag);
        HFST(5);
    }
}
extern "C" void kernel_launch(void* const* d_in, const int* in_sizes, int n_in, void* d_out, int out_size, void* d_ws, size_t ws_size, hipStream_t stream) {
    (void)hipFuncSetAttribute((const void*)fused, hipFuncAttributeMaxDynamicSharedMemorySize, LDSB);
    fused<<<512, 512, LDSB, stream>>>((const u16*)d_in[0], (const u16*)d_in[1], (const u16*)d_in[2], (const u16*)d_in[3], (const u16*)d_in[4], (const u16*)d_in[5], (const u16*)d_in[6], (const u16*)d_in[7],
                                      (const u16*)d_in[8], (const u16*)d_in[9], (const u16*)d_in[10], (const u16*)d_in[11], (u16*)d_out, (char*)d_ws);
}
```
